```python
import torch

def _softmax(x, axis=-1):
    return torch.softmax(x, dim=axis)
def _gelu(x):
    return torch.nn.functional.gelu(x, approximate="tanh")
def _silu(x):
    return torch.nn.functional.silu(x)
def _sigmoid(x):
    return torch.sigmoid(x)
def _layernorm(x, axis=-1, eps=1e-5):
    m = x.mean(dim=axis, keepdim=True)
    v = x.var(dim=axis, keepdim=True, unbiased=False)
    return (x - m) / torch.sqrt(v + eps)
def _scatter_add(tgt, idx, src, axis=0):
    return tgt.clone().index_add_(axis, idx, src)
def _segment_sum(src, seg, n):
    out = torch.zeros((n,) + src.shape[1:], dtype=src.dtype)
    return out.index_add_(0, seg, src)
def _pad(x, pw):
    flat = tuple(v for pair in reversed(pw) for v in pair)
    return torch.nn.functional.pad(x, flat)

kernel_name = 'concat_heads__sliding_block__spectral_fnet__prompt'

def setup_inputs():

    _w = (lambda a: (lambda t: t.to(torch.bfloat16) if t.is_floating_point() else t)(torch.as_tensor(a)))
    def _A_setup():
        B, S, D = 1, 2048, 1024
        return {
            "x": _w((lambda *s: torch.randn(*s, dtype=torch.bfloat16) * 0.2)(B, S, D)),
            "wqkv": _w((lambda *s: torch.randn(*s, dtype=torch.bfloat16) * 0.8)(D, 3 * D)),
            "wo": _w((lambda *s: torch.randn(*s, dtype=torch.bfloat16) * 0.2)(D, D)),
        }
    _A_in = _A_setup()

    def _B_setup():
        B, S, D = 1, 2048, 1024
        ns = torch.arange(S)[:, None] * torch.arange(S)[None, :]
        nd = torch.arange(D)[:, None] * torch.arange(D)[None, :]
        Ws_re = torch.cos(-2.0 * torch.pi * ns / S).to(float)
        Ws_im = torch.sin(-2.0 * torch.pi * ns / S).to(float)
        Wd_re = torch.cos(-2.0 * torch.pi * nd / D).to(float)
        Wd_im = torch.sin(-2.0 * torch.pi * nd / D).to(float)
        return {
            "x": _w((lambda *s: torch.randn(*s, dtype=torch.bfloat16) * 0.05)(B, S, D)),
            "dft_seq_re": _w(Ws_re),
            "dft_seq_im": _w(Ws_im),
            "dft_hid_re": _w(Wd_re),
            "dft_hid_im": _w(Wd_im),
        }
    _B_in = _B_setup()

    return {
        "A_x": _A_in["x"],
        "A_wqkv": _A_in["wqkv"],
        "A_wo": _A_in["wo"],
        "B_x": _B_in["x"],
        "B_dft_seq_re": _B_in["dft_seq_re"],
        "B_dft_seq_im": _B_in["dft_seq_im"],
        "B_dft_hid_re": _B_in["dft_hid_re"],
        "B_dft_hid_im": _B_in["dft_hid_im"],
    }

def reference(A_x, A_wqkv, A_wo, B_x, B_dft_seq_re, B_dft_seq_im, B_dft_hid_re, B_dft_hid_im):
    def _A_ref(x, wqkv, wo):
        B, S, D = x.shape
        H = 32; Dh = D // H; W = 256
        qkv = (lambda x, *d: x.permute(*d))((x @ wqkv).reshape(B, S, 3, H, Dh), 2, 0, 3, 1, 4)
        q, k, v = qkv[0], qkv[1], qkv[2]
        scores = torch.einsum("bhqd,bhkd->bhqk", q, k) * (Dh ** -0.5)
        idx = torch.arange(S)
        mask = (idx[:, None] - idx[None, :] >= 0) & (idx[:, None] - idx[None, :] < W)
        scores = torch.where(mask[None, None], scores, -1e9)
        p = _softmax(scores, axis=-1)
        o = torch.einsum("bhqk,bhkd->bhqd", p, v)
        return (lambda x, *d: x.permute(*d))(o, 0, 2, 1, 3).reshape(B, S, D) @ wo

    def _B_ref(x, dft_seq_re, dft_seq_im, dft_hid_re, dft_hid_im):
        a = torch.einsum("bsd,ed->bse", x, dft_hid_re)
        b = torch.einsum("bsd,ed->bse", x, dft_hid_im)
        y = torch.einsum("ts,bse->bte", dft_seq_re, a) - torch.einsum("ts,bse->bte", dft_seq_im, b)
        return y

    a = _A_ref(A_x, A_wqkv, A_wo)
    b = _B_ref(B_x, B_dft_seq_re, B_dft_seq_im, B_dft_hid_re, B_dft_hid_im)
    return torch.concatenate([a, b], axis=-1)
```

```cpp
#include <hip/hip_runtime.h>
#include <hip/hip_bf16.h>
#include <stdint.h>

typedef unsigned short bf16_t;
__device__ __forceinline__ float bf2f(bf16_t v) { return __uint_as_float(((unsigned)v) << 16); }
__device__ __forceinline__ bf16_t f2bf(float f) { unsigned u = __float_as_uint(f); u += 0x7fffu + ((u >> 16) & 1u); return (bf16_t)(u >> 16); }

__global__ void gemm_naive(const bf16_t* A, long sam, long sak, const bf16_t* B, long sbn, long sbk,
                           const bf16_t* A2, const bf16_t* B2, float alpha2,
                           bf16_t* C, long scm, long scn, int M, int N, int K) {
    __shared__ float As[16][17], Bs[16][17];
    int tx = threadIdx.x & 15, ty = threadIdx.x >> 4;
    int m0 = blockIdx.y * 16, n0 = blockIdx.x * 16;
    float acc = 0.f, acc2 = 0.f;
    for (int k0 = 0; k0 < K; k0 += 16) {
        As[ty][tx] = bf2f(A[(long)(m0 + ty) * sam + (long)(k0 + tx) * sak]);
        Bs[ty][tx] = bf2f(B[(long)(n0 + ty) * sbn + (long)(k0 + tx) * sbk]);
        __syncthreads();
        for (int k = 0; k < 16; ++k) acc += As[ty][k] * Bs[tx][k];
        __syncthreads();
    }
    if (A2) {
        for (int k0 = 0; k0 < K; k0 += 16) {
            As[ty][tx] = bf2f(A2[(long)(m0 + ty) * sam + (long)(k0 + tx) * sak]);
            Bs[ty][tx] = bf2f(B2[(long)(n0 + ty) * sbn + (long)(k0 + tx) * sbk]);
            __syncthreads();
            for (int k = 0; k < 16; ++k) acc2 += As[ty][k] * Bs[tx][k];
            __syncthreads();
        }
    }
    C[(long)(m0 + ty) * scm + (long)(n0 + tx) * scn] = f2bf(acc + alpha2 * acc2);
}

#define MIMIC 0
__global__ void attn_naive(const bf16_t* qkv, bf16_t* o) {
    int q = blockIdx.x, h = blockIdx.y, j = threadIdx.x;
    __shared__ float red[256];
    __shared__ float p[256];
    int k = q - j;
    float s = -1e30f;
    if (k >= 0) {
        float a = 0.f;
        for (int d = 0; d < 32; ++d) a += bf2f(qkv[(long)q * 3072 + h * 32 + d]) * bf2f(qkv[(long)k * 3072 + 1024 + h * 32 + d]);
        if (MIMIC) { a = bf2f(f2bf(a)); a = bf2f(f2bf(a * 0.17677669529663687f)); }
        else a *= 0.17677669529663687f;
        s = a;
    }
    red[j] = s; __syncthreads();
    for (int st = 128; st > 0; st >>= 1) { if (j < st) red[j] = fmaxf(red[j], red[j + st]); __syncthreads(); }
    float m = red[0]; __syncthreads();
    float e = (k >= 0) ? __expf(s - m) : 0.f;
    red[j] = e; __syncthreads();
    for (int st = 128; st > 0; st >>= 1) { if (j < st) red[j] += red[j + st]; __syncthreads(); }
    float sum = red[0];
    float pv = e / sum;
    if (MIMIC) pv = bf2f(f2bf(pv));
    p[j] = pv; __syncthreads();
    if (j < 32) {
        float a = 0.f;
        for (int i = 0; i < 256; ++i) { int kk = q - i; if (kk >= 0) a += p[i] * bf2f(qkv[(long)kk * 3072 + 2048 + h * 32 + j]); }
        o[(long)q * 1024 + h * 32 + j] = f2bf(a);
    }
}

extern "C" void kernel_launch(void* const* d_in, const int* in_sizes, int n_in,
                              void* d_out, int out_size, void* d_ws, size_t ws_size,
                              hipStream_t stream) {
    const bf16_t* Ax = (const bf16_t*)d_in[0];
    const bf16_t* Wqkv = (const bf16_t*)d_in[1];
    const bf16_t* Wo = (const bf16_t*)d_in[2];
    const bf16_t* Bx = (const bf16_t*)d_in[3];
    const bf16_t* Cr = (const bf16_t*)d_in[4];
    const bf16_t* Ci = (const bf16_t*)d_in[5];
    const bf16_t* Hr = (const bf16_t*)d_in[6];
    const bf16_t* Hi = (const bf16_t*)d_in[7];
    bf16_t* out = (bf16_t*)d_out;
    bf16_t* ws = (bf16_t*)d_ws;
    bf16_t* qkv = ws;
    bf16_t* o = qkv + 2048 * 3072;
    bf16_t* a = o + 2048 * 1024;
    bf16_t* b = a + 2048 * 1024;
    gemm_naive<<<dim3(3072 / 16, 2048 / 16), 256, 0, stream>>>(Ax, 1024, 1, Wqkv, 1, 3072, nullptr, nullptr, 0.f, qkv, 3072, 1, 2048, 3072, 1024);
    gemm_naive<<<dim3(1024 / 16, 2048 / 16), 256, 0, stream>>>(Bx, 1024, 1, Hr, 1024, 1, nullptr, nullptr, 0.f, a, 1024, 1, 2048, 1024, 1024);
    gemm_naive<<<dim3(1024 / 16, 2048 / 16), 256, 0, stream>>>(Bx, 1024, 1, Hi, 1024, 1, nullptr, nullptr, 0.f, b, 1024, 1, 2048, 1024, 1024);
    attn_naive<<<dim3(2048, 32), 256, 0, stream>>>(qkv, o);
    gemm_naive<<<dim3(1024 / 16, 2048 / 16), 256, 0, stream>>>(o, 1024, 1, Wo, 1, 1024, nullptr, nullptr, 0.f, out, 2048, 1, 2048, 1024, 1024);
    gemm_naive<<<dim3(1024 / 16, 2048 / 16), 256, 0, stream>>>(Cr, 2048, 1, a, 1, 1024, Ci, b, -1.f, out + 1024, 2048, 1, 2048, 1024, 2048);
}
```

```cpp
#include <hip/hip_runtime.h>
#include <stdint.h>

#define LAS __attribute__((address_space(3)))
typedef short bf16x8 __attribute__((ext_vector_type(8)));
typedef short s16x4 __attribute__((ext_vector_type(4)));
typedef float f32x2 __attribute__((ext_vector_type(2)));
typedef float f32x4 __attribute__((ext_vector_type(4)));
typedef __bf16 bfx2 __attribute__((ext_vector_type(2)));
typedef unsigned u32x4 __attribute__((ext_vector_type(4)));
typedef unsigned u32x2 __attribute__((ext_vector_type(2)));
typedef unsigned long long u64;
typedef __amdgpu_buffer_rsrc_t rsrc_t;

#define WAIT_V(n) asm volatile("s_waitcnt vmcnt(%0)" ::"n"(n) : "memory")
#define WAIT_L(n) asm volatile("s_waitcnt lgkmcnt(%0)" ::"n"(n) : "memory")
#define BAR() do { asm volatile("" ::: "memory"); __builtin_amdgcn_s_barrier(); asm volatile("" ::: "memory"); } while (0)
#define SCHED() __builtin_amdgcn_sched_barrier(0)
#define MFMA(a, b, c) __builtin_amdgcn_mfma_f32_16x16x32_bf16(a, b, c, 0, 0, 0)

#define STAMPS 0
#define STAMP(i) do { } while (0)
#define STAMPC(i) do { } while (0)
#define DBGP(i) nullptr

namespace {

constexpr int AUX_DEV = 16;
constexpr unsigned WS_U = 0;
constexpr unsigned WS_KV = 8u << 20;
constexpr unsigned WS_Q = 16u << 20;
constexpr unsigned WS_O = 20u << 20;
constexpr unsigned WS_FLAG = 24u << 20;
constexpr unsigned WS_STAMP = 25u << 20;
constexpr unsigned WS_BYTES = 26u << 20;
constexpr unsigned FL_EB = 0, FL_EA = 1024, FL_U = 2048, FL_O = 3072, FL_KV = 4096;
constexpr int LDS_BYTES = 160 * 1024;
constexpr int SPIN_MAX = 100000;

__device__ __forceinline__ unsigned pk(float a, float b) {
    f32x2 v = {a, b};
    return __builtin_bit_cast(unsigned, __builtin_convertvector(v, bfx2));
}
__device__ __forceinline__ u32x4 pk8(const f32x4& a, const f32x4& b) {
    u32x4 r;
    r.x = pk(a[0], a[1]); r.y = pk(a[2], a[3]); r.z = pk(b[0], b[1]); r.w = pk(b[2], b[3]);
    return r;
}
__device__ __forceinline__ rsrc_t mk(const void* p, unsigned bytes) { return __builtin_amdgcn_make_buffer_rsrc((void*)p, 0, (int)bytes, 0x00020000); }
template <int AUX>
__device__ __forceinline__ void dma(rsrc_t r, LAS char* l, unsigned vo, unsigned so) {
    __builtin_amdgcn_raw_ptr_buffer_load_lds(r, (LAS void*)l, 16, (int)vo, (int)so, 0, AUX);
}
__device__ __forceinline__ bf16x8 ld128(LAS char* p) { return *(const LAS bf16x8*)p; }
__device__ __forceinline__ s16x4 ld64(LAS char* p) { return *(const LAS s16x4*)p; }
__device__ __forceinline__ s16x4 ldtr(LAS char* p) { return __builtin_amdgcn_ds_read_tr16_b64_v4i16((LAS s16x4*)p); }
__device__ __forceinline__ bf16x8 cat(s16x4 a, s16x4 b) { return __builtin_shufflevector(a, b, 0, 1, 2, 3, 4, 5, 6, 7); }

__device__ __forceinline__ u64 mix(u64 z) {
    z += 0x9E3779B97F4A7C15ull;
    z = (z ^ (z >> 30)) * 0xBF58476D1CE4E5B9ull;
    z = (z ^ (z >> 27)) * 0x94D049BB133111EBull;
    return z ^ (z >> 31);
}
__device__ __forceinline__ u64 ld_dev(const char* p) { return __hip_atomic_load((const u64*)p, __ATOMIC_RELAXED, __HIP_MEMORY_SCOPE_AGENT); }
__device__ __forceinline__ void st_dev(char* p, u64 v) { __hip_atomic_store((u64*)p, v, __ATOMIC_RELAXED, __HIP_MEMORY_SCOPE_AGENT); }
__device__ __forceinline__ void poll(const char* base, int n, u64 tag, int lane) {
    const char* p = base + (lane & (n - 1)) * 8;
    for (int it = 0; it < SPIN_MAX; ++it) {
        const u64 v = ld_dev(p);
        if (__all(v == tag)) break;
        __builtin_amdgcn_s_sleep(2);
    }
}

template <int S, int P, int NK, class ISSUE>
__device__ __forceinline__ void loader_loop(ISSUE issue, u64* dbg = nullptr) {
#pragma unroll
    for (int s = 0; s < S; ++s) issue(s, s);
    WAIT_V((S - 1) * P);
    BAR();
    int slot = 0;
    for (int k = 0; k < NK - S; ++k) {
        WAIT_V((S - 2) * P);
        BAR();
        issue(k + S, slot);
        slot = slot == S - 1 ? 0 : slot + 1;
    }
#pragma unroll
    for (int t = 0; t < S; ++t) {
        WAIT_V((S - 2 - t > 0 ? S - 2 - t : 0) * P);
        BAR();
    }
}

template <int MT, int NT, int SWAP0, int RPG, class FA, class FB>
__device__ __forceinline__ void half_step(f32x4 (&acc)[MT][NT], const bf16x8 (&ac)[MT], const bf16x8 (&bc)[NT],
                                          bf16x8 (&an)[MT], bf16x8 (&bn)[NT], FA rdA, FB rdB) {
    s16x4 lo[NT];
    constexpr int GB = (2 * NT + RPG - 1) / RPG;
#pragma unroll
    for (int m = 0; m < MT; ++m) {
#pragma unroll
        for (int n = 0; n < NT; ++n) {
            const int i = m * NT + n;
            acc[m][n] = (n >= SWAP0) ? MFMA(ac[m], bc[n], acc[m][n]) : MFMA(bc[n], ac[m], acc[m][n]);
            SCHED();
            if (i < GB) {
#pragma unroll
                for (int j = 0; j < RPG; ++j) {
                    const int o = i * RPG + j;
                    if (o < 2 * NT) {
                        if ((o & 1) == 0) lo[o >> 1] = rdB(o >> 1, 0);
                        else bn[o >> 1] = cat(lo[o >> 1], rdB(o >> 1, 1));
                    }
                }
                SCHED();
            } else if (i - GB < MT) {
                an[i - GB] = rdA(i - GB);
                SCHED();
            }
        }
    }
}

template <int MT, int NT, int SWAP0, int RPG, int NK, int S, int STAGE, class FA, class FB>
__device__ __forceinline__ void compute_loop(f32x4 (&acc)[MT][NT], bf16x8 (&a0)[MT], bf16x8 (&b0)[NT], int& slot, FA rdA, FB rdB) {
    bf16x8 a1[MT], b1[NT];
    for (int k = 0; k < NK; ++k) {
        const int so = slot * STAGE;
        const int nslot = slot == S - 1 ? 0 : slot + 1;
        const int sn = nslot * STAGE;
        SCHED();
        half_step<MT, NT, SWAP0, RPG>(acc, a0, b0, a1, b1, [&](int m) { return rdA(m, 1, so); }, [&](int n, int t) { return rdB(n, 1, t, so); });
        WAIT_L(0);
        BAR();
        SCHED();
        half_step<MT, NT, SWAP0, RPG>(acc, a1, b1, a0, b0, [&](int m) { return rdA(m, 0, sn); }, [&](int n, int t) { return rdB(n, 0, t, sn); });
        slot = nslot;
    }
}
template <int MT, int NT, class FA, class FB>
__device__ __forceinline__ void first_frags(bf16x8 (&a0)[MT], bf16x8 (&b0)[NT], FA rdA, FB rdB) {
#pragma unroll
    for (int n = 0; n < NT; ++n) b0[n] = cat(rdB(n, 0, 0, 0), rdB(n, 0, 1, 0));
#pragma unroll
    for (int m = 0; m < MT; ++m) a0[m] = rdA(m, 0, 0);
}
template <int MT, int NT>
__device__ __forceinline__ void zero_acc(f32x4 (&acc)[MT][NT]) {
#pragma unroll
    for (int m = 0; m < MT; ++m)
#pragma unroll
        for (int n = 0; n < NT; ++n) acc[m][n] = (f32x4){0.f, 0.f, 0.f, 0.f};
}

__device__ __forceinline__ unsigned vo_rows(int lane, unsigned ld) { return (unsigned)(lane >> 3) * ld + (unsigned)(((lane & 7) ^ (lane >> 4)) * 16); }
__device__ __forceinline__ unsigned vo_tr(int lane, unsigned rowoff) { return rowoff + (unsigned)(((lane & 7) ^ (((lane >> 4) & 1) << 1)) * 16); }
__device__ __forceinline__ int tr_base(int lane, int c) {
    const int g = lane >> 4, q = (lane & 15) >> 2, p = lane & 3;
    const int f = ((q >> 1) & 1) | ((g & 1) << 1);
    return 128 * (8 * g + q) + ((((c ^ f) << 1) | (p >> 1)) * 16) + 8 * (p & 1);
}

__device__ __forceinline__ void stage_write(LAS char* lds, const f32x4 (&acc)[4][4], int wm, int wn, int lane) {
    const int fr = lane & 15, g = lane >> 4;
#pragma unroll
    for (int m = 0; m < 4; ++m)
#pragma unroll
        for (int n = 0; n < 4; ++n) {
            const int row = wm * 64 + m * 16 + fr;
            const int ch = (wn * 8 + 2 * n + (g >> 1)) ^ ((fr >> 1) & 7);
            u32x2 w;
            w.x = pk(acc[m][n][0], acc[m][n][1]);
            w.y = pk(acc[m][n][2], acc[m][n][3]);
            *(LAS u32x2*)(lds + row * 256 + ch * 16 + 8 * (g & 1)) = w;
        }
}
__device__ __forceinline__ void stage_store(LAS char* lds, char* dst, int wid, int lane) {
#pragma unroll
    for (int i = 0; i < 4; ++i) {
        const int row = wid * 16 + i * 4 + (lane >> 4);
        const int ch = (lane & 15) ^ ((row >> 1) & 7);
        const u32x4 v = *(const LAS u32x4*)(lds + row * 256 + ch * 16);
        *(u32x4*)(dst + (size_t)row * 4096 + (lane & 15) * 16) = v;
    }
}

__device__ __forceinline__ void chain_b(LAS char* lds, int g, int i16, int wid, int lane, const char* bx, const char* cr, const char* ci,
                                        const char* hr, const char* hi, char* out, char* ws) {
    const int part = i16 >> 3, dt = i16 & 7;
    const int hh = i16 & 1, et = i16 >> 1;
    char* fl = ws + WS_FLAG;
    constexpr int ST1 = 49152, ST2 = 32768;
    STAMP(0);
    if (wid >= 4) {
        const int w4 = wid - 4;
        u64 ep = 0;
        if (w4 == 0) ep = ld_dev(fl + FL_EB + g * 128);
        {
            const rsrc_t rc = mk(part ? ci : cr, 2048 * 2048 * 2), rx = mk(bx, 2048 * 1024 * 2);
            const unsigned va0 = vo_rows(lane, 4096), va1 = va0 ^ 64;
            const unsigned vb0 = vo_tr(lane, (lane >> 3) * 2048), vb1 = vb0 ^ 64;
            const unsigned sa = (unsigned)(g * 256 + w4 * 64) * 4096;
            const unsigned sb = (unsigned)((w4 & 1) * 32) * 2048 + (unsigned)(dt * 128 + (w4 >> 1) * 64) * 2;
            loader_loop<3, 12, 32>([&](int k, int slot) {
                LAS char* l = lds + slot * ST1;
#pragma unroll
                for (int j = 0; j < 4; ++j) dma<0>(rx, l + 32768 + (w4 * 4 + j) * 1024, (j & 1) ? vb1 : vb0, sb + (unsigned)(k * 64 + j * 8) * 2048);
#pragma unroll
                for (int j = 0; j < 8; ++j) dma<0>(rc, l + (w4 * 8 + j) * 1024, (j & 1) ? va1 : va0, sa + (unsigned)j * 8 * 4096 + (unsigned)k * 128);
            }, DBGP(4));
        }
        STAMP(1);
        BAR();
        if (w4 == 0) {
            const u64 tag = mix(ep);
            if (lane == 0) st_dev(fl + FL_U + g * 128 + i16 * 8, tag);
            poll(fl + FL_U + g * 128, 16, tag, lane);
            if (i16 == 0 && lane == 0) st_dev(fl + FL_EB + g * 128, tag);
        }
        STAMP(2);
        BAR();
        {
            const rsrc_t ru = mk(ws + WS_U, 8u << 20), rh0 = mk(hr, 1024 * 1024 * 2), rh1 = mk(hi, 1024 * 1024 * 2);
            const unsigned vl = lane * 16;
            const unsigned vb0 = vo_rows(lane, 2048), vb1 = vb0 ^ 64;
            const unsigned su = (unsigned)(g * 16 + hh * 8 + w4 * 2) * 65536;
            const unsigned sb = (unsigned)(et * 128 + w4 * 32) * 2048;
            loader_loop<4, 8, 32>([&](int k, int slot) {
                LAS char* l = lds + slot * ST2;
                const rsrc_t rh = k < 16 ? rh0 : rh1;
#pragma unroll
                for (int j = 0; j < 4; ++j) dma<0>(rh, l + 16384 + (w4 * 4 + j) * 1024, (j & 1) ? vb1 : vb0, sb + (unsigned)j * 8 * 2048 + (unsigned)(k & 15) * 128);
#pragma unroll
                for (int j = 0; j < 4; ++j) dma<AUX_DEV>(ru, l + (w4 * 4 + j) * 1024, vl, su + (unsigned)(j >> 1) * 65536 + (unsigned)(k * 2 + (j & 1)) * 1024);
            }, DBGP(10));
        }
        STAMP(3);
        BAR();
        stage_store(lds, out + (size_t)(g * 256 + hh * 128) * 4096 + 2048 + et * 256, wid, lane);
    } else {
        const int wm = wid >> 1, wn = wid & 1, fr = lane & 15, fq = lane >> 4;
        {
            f32x4 acc[8][4];
            bf16x8 a0[8], b0[4];
            zero_acc(acc);
            const int ra0 = (wm * 128 + fr) * 128 + ((fq ^ (fr >> 1)) * 16), ra1 = ra0 ^ 64;
            int rb[4];
#pragma unroll
            for (int c = 0; c < 4; ++c) rb[c] = 32768 + wn * 8192 + tr_base(lane, c);
            auto rdA = [&](int m, int kh, int so) { return ld128(lds + so + (kh ? ra1 : ra0) + m * 2048); };
            auto rdB = [&](int n, int kh, int t, int so) { return ldtr(lds + so + rb[n] + kh * 4096 + t * 512); };
            BAR();
            STAMPC(4);
            first_frags(a0, b0, rdA, rdB);
            int slot = 0;
            compute_loop<8, 4, 4, 1, 32, 3, ST1>(acc, a0, b0, slot, rdA, rdB);
            STAMPC(5);
            const rsrc_t ru = mk(ws + WS_U, 8u << 20);
            const unsigned sg = part ? 0x80008000u : 0u;
            const unsigned so = (unsigned)((g * 16 + wm * 8) * 64 + part * 32 + dt * 4 + wn * 2) * 1024;
#pragma unroll
            for (int m = 0; m < 8; ++m)
#pragma unroll
                for (int np = 0; np < 2; ++np) {
                    u32x4 v = pk8(acc[m][2 * np], acc[m][2 * np + 1]);
                    v.x ^= sg; v.y ^= sg; v.z ^= sg; v.w ^= sg;
                    __builtin_amdgcn_raw_buffer_store_b128(v, ru, lane * 16, so + (unsigned)(m * 64 + np) * 1024, AUX_DEV);
                }
            WAIT_V(0);
            STAMP(6);
        }
        BAR();
        BAR();
        {
            f32x4 acc[4][4];
            bf16x8 a0[4], b0[4];
            zero_acc(acc);
            const int ra = wm * 8192 + lane * 16;
            int rb[4];
#pragma unroll
            for (int c = 0; c < 4; ++c) rb[c] = 16384 + (wn * 64 + fr) * 128 + (((c * 2 + (fq >> 1)) ^ (fr >> 1)) * 16) + 8 * (fq & 1);
            auto rdA = [&](int m, int kh, int so) { return ld128(lds + so + ra + m * 2048 + kh * 1024); };
            auto rdB = [&](int n, int kh, int t, int so) { return ld64(lds + so + rb[kh * 2 + t] + n * 2048); };
            BAR();
            STAMPC(7);
            first_frags(a0, b0, rdA, rdB);
            int slot = 0;
            compute_loop<4, 4, 4, 2, 32, 4, ST2>(acc, a0, b0, slot, rdA, rdB);
            STAMPC(8);
            stage_write(lds, acc, wm, wn, lane);
            WAIT_L(0);
        }
        BAR();
        stage_store(lds, out + (size_t)(g * 256 + hh * 128) * 4096 + 2048 + et * 256, wid, lane);
    }
    STAMP(9);
}

template <bool FIRST, int PAR>
__device__ __forceinline__ u32x4 attn_strip(LAS char* img, int s, bf16x8 qf, int lane) {
    constexpr float C = 0.17677669529663687f * 1.4426950408889634f;
    constexpr int JL = PAR, JD = PAR + 16;
    LAS char* p = img + (s & ~1) * 4096;
    const int r = lane & 15, g4 = (lane >> 4) * 4;
    f32x4 sc[18];
#pragma unroll
    for (int j = JL; j <= JD; ++j) sc[j] = MFMA(ld128(p + j * 4096), qf, ((f32x4){0.f, 0.f, 0.f, 0.f}));
    const float ninf = -__builtin_inff();
    f32x4 mg;
#pragma unroll
    for (int e = 0; e < 4; ++e) mg[e] = (g4 + e > r) ? (FIRST ? ninf : sc[JL][e]) : sc[JD][e];
    if (FIRST) {
#pragma unroll
        for (int j = JL + 1; j < JD; ++j) {
            const bool bad = (s & ~1) + j < 16;
#pragma unroll
            for (int e = 0; e < 4; ++e) sc[j][e] = bad ? ninf : sc[j][e];
        }
    }
    float mx = fmaxf(fmaxf(mg[0], mg[1]), fmaxf(mg[2], mg[3]));
#pragma unroll
    for (int j = JL + 1; j < JD; ++j) {
        mx = fmaxf(fmaxf(mx, sc[j][0]), sc[j][1]);
        mx = fmaxf(fmaxf(mx, sc[j][2]), sc[j][3]);
    }
    {
        auto a = __builtin_amdgcn_permlane16_swap(__float_as_uint(mx), __float_as_uint(mx), false, false);
        mx = fmaxf(__uint_as_float(a[0]), __uint_as_float(a[1]));
        auto b = __builtin_amdgcn_permlane32_swap(__float_as_uint(mx), __float_as_uint(mx), false, false);
        mx = fmaxf(__uint_as_float(b[0]), __uint_as_float(b[1]));
    }
    const float mc = -mx * C;
#pragma unroll
    for (int e = 0; e < 4; ++e) mg[e] = __builtin_amdgcn_exp2f(fmaf(mg[e], C, mc));
#pragma unroll
    for (int j = JL + 1; j < JD; ++j)
#pragma unroll
        for (int e = 0; e < 4; ++e) sc[j][e] = __builtin_amdgcn_exp2f(fmaf(sc[j][e], C, mc));
#pragma unroll
    for (int e = 0; e < 4; ++e) {
        const bool lo = g4 + e > r;
        sc[JL][e] = lo ? mg[e] : 0.f;
        sc[JD][e] = lo ? 0.f : mg[e];
    }
    sc[PAR ? 0 : 17] = (f32x4){0.f, 0.f, 0.f, 0.f};
    f32x4 o0 = {0.f, 0.f, 0.f, 0.f}, o1 = o0, sum = o0;
    const bf16x8 ones = {0x3F80, 0x3F80, 0x3F80, 0x3F80, 0x3F80, 0x3F80, 0x3F80, 0x3F80};
#pragma unroll
    for (int j2 = 0; j2 < 9; ++j2) {
        const bf16x8 pf = __builtin_bit_cast(bf16x8, pk8(sc[2 * j2], sc[2 * j2 + 1]));
        o0 = MFMA(ld128(p + (2 * j2) * 4096 + 2048), pf, o0);
        o1 = MFMA(ld128(p + (2 * j2 + 1) * 4096 + 2048), pf, o1);
        sum = MFMA(ones, pf, sum);
    }
    const float inv = 1.0f / sum[0];
    o0 *= inv;
    o1 *= inv;
    return pk8(o0, o1);
}

template <bool FIRST>
__device__ __forceinline__ void attention(LAS char* lds, int r, int hp, int wid, int lane, char* ws) {
    const int h = wid & 1, sp = wid >> 1;
    const int head = hp * 2 + h;
    const rsrc_t rq = mk(ws + WS_Q, 4u << 20), ro = mk(ws + WS_O, 4u << 20);
    LAS char* img = lds + 32768 + h * 1024 + lane * 16;
    bf16x8 qf[4];
#pragma unroll
    for (int i = 0; i < 4; ++i) {
        const int s = (i < 2 ? 8 : 0) + sp * 2 + (i & 1);
        qf[i] = __builtin_bit_cast(bf16x8, __builtin_amdgcn_raw_buffer_load_b128(rq, lane * 16, (unsigned)(head * 128 + r * 16 + s) * 1024, AUX_DEV));
    }
    WAIT_V(0);
    BAR();
#pragma unroll
    for (int i = 0; i < 2; ++i) {
        const int s0 = (i == 0 ? 8 : 0) + sp * 2;
        const u32x4 v0 = attn_strip<FIRST, 0>(img, s0, qf[2 * i], lane);
        __builtin_amdgcn_raw_buffer_store_b128(v0, ro, lane * 16, (unsigned)((r * 16 + s0) * 32 + head) * 1024, AUX_DEV);
        const u32x4 v1 = attn_strip<FIRST, 1>(img, s0 + 1, qf[2 * i + 1], lane);
        __builtin_amdgcn_raw_buffer_store_b128(v1, ro, lane * 16, (unsigned)((r * 16 + s0 + 1) * 32 + head) * 1024, AUX_DEV);
    }
    WAIT_V(0);
}

__device__ __forceinline__ void chain_a(LAS char* lds, int r, int hp, int wid, int lane, const char* ax, const char* wqkv, const char* wo,
                                        char* out, char* ws) {
    char* fl = ws + WS_FLAG;
    constexpr int ST1 = 40960, ST3 = 32768;
    u64 ep0 = 0, ep1 = 0;
    STAMP(0);
    if (wid >= 4) {
        const int w4 = wid - 4;
        if (w4 == 0) {
            ep0 = ld_dev(fl + FL_EA + r * 128);
            ep1 = ld_dev(fl + FL_EA + ((r + 1) & 7) * 128);
        }
        const rsrc_t rx = mk(ax, 2048 * 1024 * 2), rw = mk(wqkv, 1024 * 3072 * 2);
        const unsigned va0 = vo_rows(lane, 2048), va1 = va0 ^ 64;
        const unsigned vb0 = vo_tr(lane, (lane >> 3) * 6144), vb1 = vb0 ^ 64;
        const unsigned sa = (unsigned)(r * 256 + w4 * 32) * 2048;
        unsigned sb[6];
#pragma unroll
        for (int j = 0; j < 6; ++j) {
            const int i = w4 * 6 + j;
            sb[j] = (unsigned)((i & 7) * 8) * 6144 + (unsigned)((i >> 3) * 1024 + hp * 64) * 2;
        }
        loader_loop<3, 10, 32>([&](int k, int slot) {
            LAS char* l = lds + slot * ST1;
            const unsigned kk = k & 15, sub = (k >> 4) ^ 1;
#pragma unroll
            for (int j = 0; j < 6; ++j) dma<0>(rw, l + 16384 + (w4 * 6 + j) * 1024, (j & 1) ? vb1 : vb0, sb[j] + kk * 64 * 6144);
#pragma unroll
            for (int j = 0; j < 4; ++j) dma<0>(rx, l + (w4 * 4 + j) * 1024, (j & 1) ? va1 : va0, sa + (sub * 128 + j * 8) * 2048 + kk * 128);
        }, DBGP(4));
        STAMP(1);
    } else {
        const int wm = wid >> 1, h = wid & 1, fr = lane & 15, fq = lane >> 4;
        f32x4 acc[4][6];
        bf16x8 a0[4], b0[6];
        zero_acc(acc);
        const int ra0 = (wm * 64 + fr) * 128 + ((fq ^ (fr >> 1)) * 16), ra1 = ra0 ^ 64;
        const int rb0 = 16384 + tr_base(lane, 2 * h), rb1 = 16384 + tr_base(lane, 2 * h + 1);
        auto rdA = [&](int m, int kh, int so) { return ld128(lds + so + (kh ? ra1 : ra0) + m * 2048); };
        auto rdB = [&](int n, int kh, int t, int so) { return ldtr(lds + so + ((n & 1) ? rb1 : rb0) + (n >> 1) * 8192 + kh * 4096 + t * 512); };
        const rsrc_t rq = mk(ws + WS_Q, 4u << 20), rkv = mk(ws + WS_KV, 8u << 20);
        BAR();
        STAMPC(4);
        first_frags(a0, b0, rdA, rdB);
        int slot = 0;
#pragma unroll 1
        for (int it = 0; it < 2; ++it) {
            compute_loop<4, 6, 4, 1, 16, 3, ST1>(acc, a0, b0, slot, rdA, rdB);
            const int t0 = r * 16 + (it ^ 1) * 8 + wm * 4;
            const unsigned sq = (unsigned)((hp * 2 + h) * 128 + t0) * 1024;
            const unsigned sk = (unsigned)(hp * 128 + t0) * 4096 + (unsigned)h * 1024;
#pragma unroll
            for (int m = 0; m < 4; ++m) {
                __builtin_amdgcn_raw_buffer_store_b128(pk8(acc[m][2], acc[m][3]), rkv, lane * 16, sk + m * 4096, AUX_DEV);
                if (m & 1) {
                    __builtin_amdgcn_raw_buffer_store_b128(pk8(acc[m - 1][4], acc[m][4]), rkv, lane * 16, sk + (m - 1) * 4096 + 2048, AUX_DEV);
                    __builtin_amdgcn_raw_buffer_store_b128(pk8(acc[m - 1][5], acc[m][5]), rkv, lane * 16, sk + m * 4096 + 2048, AUX_DEV);
                }
                __builtin_amdgcn_raw_buffer_store_b128(pk8(acc[m][0], acc[m][1]), rq, lane * 16, sq + m * 1024, AUX_DEV);
            }
            zero_acc(acc);
        }
        STAMPC(5);
        WAIT_V(0);
        STAMP(6);
    }
    BAR();
    {
        const rsrc_t rkv = mk(ws + WS_KV, 8u << 20);
        const unsigned vl = lane * 16;
        const unsigned own = (unsigned)(hp * 128 + r * 16) * 4096;
        if (wid == 4) {
            if (lane == 0 && r < 7) st_dev(fl + FL_KV + r * 128 + hp * 8, mix(ep1));
        }
#pragma unroll
        for (int j = 0; j < 8; ++j) dma<AUX_DEV>(rkv, lds + 32768 + 65536 + (wid * 8 + j) * 1024, vl, own + (unsigned)(wid * 8 + j) * 1024);
        if (r > 0) {
            if (wid == 4) poll(fl + FL_KV + (r - 1) * 128 + hp * 8, 1, mix(ep0), lane);
            BAR();
#pragma unroll
            for (int j = 0; j < 8; ++j) dma<AUX_DEV>(rkv, lds + 32768 + (wid * 8 + j) * 1024, vl, own - 65536 + (unsigned)(wid * 8 + j) * 1024);
            STAMP(2);
            attention<false>(lds, r, hp, wid, lane, ws);
        } else {
#pragma unroll
            for (int j = 0; j < 8; ++j) *(LAS u32x4*)(lds + 32768 + (wid * 8 + j) * 1024 + vl) = (u32x4){0u, 0u, 0u, 0u};
            WAIT_L(0);
            STAMP(2);
            attention<true>(lds, r, hp, wid, lane, ws);
        }
    }
    STAMP(3);
    BAR();
    const int hh = hp & 1, ct = hp >> 1;
    char* dst = out + (size_t)(r * 256 + hh * 128) * 4096 + ct * 256;
    if (wid >= 4) {
        const int w4 = wid - 4;
        if (w4 == 0) {
            const u64 tag = mix(ep0);
            if (lane == 0) st_dev(fl + FL_O + r * 128 + hp * 8, tag);
            poll(fl + FL_O + r * 128, 16, tag, lane);
            if (hp == 0 && lane == 0) st_dev(fl + FL_EA + r * 128, tag);
        }
        STAMP(7);
        BAR();
        const rsrc_t ro = mk(ws + WS_O, 4u << 20), rw = mk(wo, 1024 * 1024 * 2);
        const unsigned vl = lane * 16;
        const unsigned vb0 = vo_tr(lane, (unsigned)(16 * (lane >> 5) + ((lane >> 3) & 3)) * 2048), vb1 = vb0 ^ 64;
        const unsigned so = (unsigned)(r * 16 + hh * 8 + w4 * 2) * 32768;
        const unsigned sb = (unsigned)((w4 & 1) * 32) * 2048 + (unsigned)(ct * 128 + (w4 >> 1) * 64) * 2;
        loader_loop<4, 8, 16>([&](int k, int slot) {
            LAS char* l = lds + slot * ST3;
#pragma unroll
            for (int j = 0; j < 4; ++j) dma<0>(rw, l + 16384 + (w4 * 4 + j) * 1024, (j & 1) ? vb1 : vb0, sb + (unsigned)(k * 64 + j * 4) * 2048);
#pragma unroll
            for (int j = 0; j < 4; ++j) dma<AUX_DEV>(ro, l + (w4 * 4 + j) * 1024, vl, so + (unsigned)(j >> 1) * 32768 + (unsigned)(k * 2 + (j & 1)) * 1024);
        }, DBGP(10));
        STAMP(8);
        BAR();
        stage_store(lds, dst, wid, lane);
    } else {
        const int wm = wid >> 1, wn = wid & 1;
        f32x4 acc[4][4];
        bf16x8 a0[4], b0[4];
        zero_acc(acc);
        const int ra = wm * 8192 + lane * 16;
        int rb[4];
#pragma unroll
        for (int c = 0; c < 4; ++c) rb[c] = 16384 + wn * 8192 + tr_base(lane, c);
        auto rdA = [&](int m, int kh, int so) { return ld128(lds + so + ra + m * 2048 + kh * 1024); };
        auto rdB = [&](int n, int kh, int t, int so) { return ldtr(lds + so + rb[n] + kh * 4096 + t * 512); };
        BAR();
        BAR();
        STAMPC(10);
        first_frags(a0, b0, rdA, rdB);
        int slot = 0;
        compute_loop<4, 4, 4, 2, 16, 4, ST3>(acc, a0, b0, slot, rdA, rdB);
        STAMPC(11);
        stage_write(lds, acc, wm, wn, lane);
        WAIT_L(0);
        BAR();
        stage_store(lds, dst, wid, lane);
    }
    STAMP(9);
}

__global__ __launch_bounds__(512, 2) void fused_kernel(const char* ax, const char* wqkv, const char* wo, const char* bx, const char* cr,
                                                       const char* ci, const char* hr, const char* hi, char* out, char* ws) {
    extern __shared__ __attribute__((aligned(16))) char smem[];
    LAS char* lds = (LAS char*)smem;
    const int wid = __builtin_amdgcn_readfirstlane(threadIdx.x >> 6), lane = threadIdx.x & 63;
    const int x = blockIdx.x & 7, idx = blockIdx.x >> 3;
#define HPROT 0
    if (idx & 1) chain_a(lds, x, ((idx >> 1) + HPROT) & 15, wid, lane, ax, wqkv, wo, out, ws);
    else chain_b(lds, x, idx >> 1, wid, lane, bx, cr, ci, hr, hi, out, ws);
}

}

extern "C" void kernel_launch(void* const* d_in, const int* in_sizes, int n_in, void* d_out, int out_size, void* d_ws, size_t ws_size,
                              hipStream_t stream) {
    (void)in_sizes; (void)n_in; (void)out_size; (void)ws_size;
    (void)hipFuncSetAttribute((const void*)fused_kernel, hipFuncAttributeMaxDynamicSharedMemorySize, LDS_BYTES);
    fused_kernel<<<256, 512, LDS_BYTES, stream>>>((const char*)d_in[0], (const char*)d_in[1], (const char*)d_in[2], (const char*)d_in[3],
                                                  (const char*)d_in[4], (const char*)d_in[5], (const char*)d_in[6], (const char*)d_in[7],
                                                  (char*)d_out, (char*)d_ws);
}
```

```cpp
#include <hip/hip_runtime.h>
#include <stdint.h>

#define LAS __attribute__((address_space(3)))
typedef short bf16x8 __attribute__((ext_vector_type(8)));
typedef short s16x4 __attribute__((ext_vector_type(4)));
typedef float f32x2 __attribute__((ext_vector_type(2)));
typedef float f32x4 __attribute__((ext_vector_type(4)));
typedef __bf16 bfx2 __attribute__((ext_vector_type(2)));
typedef unsigned u32x4 __attribute__((ext_vector_type(4)));
typedef unsigned u32x2 __attribute__((ext_vector_type(2)));
typedef unsigned long long u64;
typedef __amdgpu_buffer_rsrc_t rsrc_t;

#define WAIT_V(n) asm volatile("s_waitcnt vmcnt(%0)" ::"n"(n) : "memory")
#define WAIT_L(n) asm volatile("s_waitcnt lgkmcnt(%0)" ::"n"(n) : "memory")
#define BAR() do { asm volatile("" ::: "memory"); __builtin_amdgcn_s_barrier(); asm volatile("" ::: "memory"); } while (0)
#define SCHED() __builtin_amdgcn_sched_barrier(0)
#define MFMA(a, b, c) __builtin_amdgcn_mfma_f32_16x16x32_bf16(a, b, c, 0, 0, 0)

#define STAMPS 0
#define MAPA 2
#define STAMP(i) do { } while (0)
#define STAMPC(i) do { } while (0)
#define DBGP(i) nullptr

namespace {

constexpr int AUX_DEV = 16;
constexpr unsigned WS_U = 0;
constexpr unsigned WS_KV = 8u << 20;
constexpr unsigned WS_Q = 16u << 20;
constexpr unsigned WS_O = 20u << 20;
constexpr unsigned WS_FLAG = 24u << 20;
constexpr unsigned WS_STAMP = 25u << 20;
constexpr unsigned WS_BYTES = 26u << 20;
constexpr unsigned FL_EB = 0, FL_EA = 1024, FL_U = 2048, FL_O = 3072, FL_KV = 4096;
constexpr int LDS_BYTES = 160 * 1024;
constexpr int SPIN_MAX = 100000;

__device__ __forceinline__ unsigned pk(float a, float b) {
    f32x2 v = {a, b};
    return __builtin_bit_cast(unsigned, __builtin_convertvector(v, bfx2));
}
__device__ __forceinline__ u32x4 pk8(const f32x4& a, const f32x4& b) {
    u32x4 r;
    r.x = pk(a[0], a[1]); r.y = pk(a[2], a[3]); r.z = pk(b[0], b[1]); r.w = pk(b[2], b[3]);
    return r;
}
__device__ __forceinline__ rsrc_t mk(const void* p, unsigned bytes) { return __builtin_amdgcn_make_buffer_rsrc((void*)p, 0, (int)bytes, 0x00020000); }
template <int AUX>
__device__ __forceinline__ void dma(rsrc_t r, LAS char* l, unsigned vo, unsigned so) {
    __builtin_amdgcn_raw_ptr_buffer_load_lds(r, (LAS void*)l, 16, (int)vo, (int)so, 0, AUX);
}
template <int AUX>
__device__ __forceinline__ void st128(u32x4 v, rsrc_t r, unsigned vo) { __builtin_amdgcn_raw_buffer_store_b128(v, r, (int)vo, 0, AUX); }
__device__ __forceinline__ bf16x8 ld128(LAS char* p) { return *(const LAS bf16x8*)p; }
__device__ __forceinline__ s16x4 ld64(LAS char* p) { return *(const LAS s16x4*)p; }
__device__ __forceinline__ s16x4 ldtr(LAS char* p) { return __builtin_amdgcn_ds_read_tr16_b64_v4i16((LAS s16x4*)p); }
__device__ __forceinline__ bf16x8 cat(s16x4 a, s16x4 b) { return __builtin_shufflevector(a, b, 0, 1, 2, 3, 4, 5, 6, 7); }

#define TD 0
#define TDA TD
#define AUX_C 0
#define AUX_AX 0
#define AUX_X 0
#define AUX_H 0
#define AUX_W 0
#define AUX_WO 0
#define AUX_I 16
__device__ __forceinline__ u32x4 srd(const void* p, unsigned bytes) {
    const u64 a = (u64)p;
    u32x4 r;
    r.x = __builtin_amdgcn_readfirstlane((unsigned)a);
    r.y = __builtin_amdgcn_readfirstlane((unsigned)(a >> 32));
    r.z = bytes;
    r.w = 0x00020000u;
    return r;
}
template <int NT>
__device__ __forceinline__ void touch(unsigned& sink, unsigned vo, const u32x4& r, unsigned so) {
    if (NT) asm volatile("buffer_load_dword %0, %1, %2, %3 offen nt" : "+v"(sink) : "v"(vo), "s"(r), "s"(so));
    else asm volatile("buffer_load_dword %0, %1, %2, %3 offen" : "+v"(sink) : "v"(vo), "s"(r), "s"(so));
}
__device__ __forceinline__ void touch_done(unsigned& sink) { asm volatile("s_waitcnt vmcnt(0)" : "+v"(sink)::"memory"); }

__device__ __forceinline__ u64 mix(u64 z) {
    z += 0x9E3779B97F4A7C15ull;
    z = (z ^ (z >> 30)) * 0xBF58476D1CE4E5B9ull;
    z = (z ^ (z >> 27)) * 0x94D049BB133111EBull;
    return z ^ (z >> 31);
}
__device__ __forceinline__ u64 ld_dev(const char* p) { return __hip_atomic_load((const u64*)p, __ATOMIC_RELAXED, __HIP_MEMORY_SCOPE_AGENT); }
__device__ __forceinline__ void st_dev(char* p, u64 v) { __hip_atomic_store((u64*)p, v, __ATOMIC_RELAXED, __HIP_MEMORY_SCOPE_AGENT); }
__device__ __forceinline__ void poll(const char* base, int n, u64 tag, int lane) {
    const char* p = base + (lane & (n - 1)) * 8;
    for (int it = 0; it < SPIN_MAX; ++it) {
        const u64 v = ld_dev(p);
        if (__all(v == tag)) break;
        __builtin_amdgcn_s_sleep(2);
    }
}

template <int S, int P, int NK, class ISSUE>
__device__ __forceinline__ void loader_loop(ISSUE issue, u64* dbg = nullptr) {
#pragma unroll
    for (int s = 0; s < S; ++s) issue(s, s);
    WAIT_V((S - 1) * P);
    BAR();
    int slot = 0;
    for (int k = 0; k < NK - S; ++k) {
        WAIT_V((S - 2) * P);
        BAR();
        issue(k + S, slot);
        slot = slot == S - 1 ? 0 : slot + 1;
    }
#pragma unroll
    for (int t = 0; t < S; ++t) {
        WAIT_V((S - 2 - t > 0 ? S - 2 - t : 0) * P);
        BAR();
    }
}

template <int MT, int NT, int SWAP0, int RPG, class FA, class FB>
__device__ __forceinline__ void half_step(f32x4 (&acc)[MT][NT], const bf16x8 (&ac)[MT], const bf16x8 (&bc)[NT],
                                          bf16x8 (&an)[MT], bf16x8 (&bn)[NT], FA rdA, FB rdB) {
    s16x4 lo[NT];
    constexpr int GB = (2 * NT + RPG - 1) / RPG;
#pragma unroll
    for (int m = 0; m < MT; ++m) {
#pragma unroll
        for (int n = 0; n < NT; ++n) {
            const int i = m * NT + n;
            acc[m][n] = (n >= SWAP0) ? MFMA(ac[m], bc[n], acc[m][n]) : MFMA(bc[n], ac[m], acc[m][n]);
            SCHED();
            if (i < GB) {
#pragma unroll
                for (int j = 0; j < RPG; ++j) {
                    const int o = i * RPG + j;
                    if (o < 2 * NT) {
                        if ((o & 1) == 0) lo[o >> 1] = rdB(o >> 1, 0);
                        else bn[o >> 1] = cat(lo[o >> 1], rdB(o >> 1, 1));
                    }
                }
                SCHED();
            } else if (i - GB < MT) {
                an[i - GB] = rdA(i - GB);
                SCHED();
            }
        }
    }
}

template <int MT, int NT, int SWAP0, int RPG, int NK, int S, int STAGE, class FA, class FB, class PF>
__device__ __forceinline__ void compute_loop(f32x4 (&acc)[MT][NT], bf16x8 (&a0)[MT], bf16x8 (&b0)[NT], int& slot, FA rdA, FB rdB, PF pf) {
    bf16x8 a1[MT], b1[NT];
    for (int k = 0; k < NK; ++k) {
        const int so = slot * STAGE;
        const int nslot = slot == S - 1 ? 0 : slot + 1;
        const int sn = nslot * STAGE;
        SCHED();
        half_step<MT, NT, SWAP0, RPG>(acc, a0, b0, a1, b1, [&](int m) { return rdA(m, 1, so); }, [&](int n, int t) { return rdB(n, 1, t, so); });
        WAIT_L(0);
        BAR();
        pf(k);
        SCHED();
        half_step<MT, NT, SWAP0, RPG>(acc, a1, b1, a0, b0, [&](int m) { return rdA(m, 0, sn); }, [&](int n, int t) { return rdB(n, 0, t, sn); });
        slot = nslot;
    }
}
template <int MT, int NT, class FA, class FB>
__device__ __forceinline__ void first_frags(bf16x8 (&a0)[MT], bf16x8 (&b0)[NT], FA rdA, FB rdB) {
#pragma unroll
    for (int n = 0; n < NT; ++n) b0[n] = cat(rdB(n, 0, 0, 0), rdB(n, 0, 1, 0));
#pragma unroll
    for (int m = 0; m < MT; ++m) a0[m] = rdA(m, 0, 0);
}
template <int MT, int NT>
__device__ __forceinline__ void zero_acc(f32x4 (&acc)[MT][NT]) {
#pragma unroll
    for (int m = 0; m < MT; ++m)
#pragma unroll
        for (int n = 0; n < NT; ++n) acc[m][n] = (f32x4){0.f, 0.f, 0.f, 0.f};
}

__device__ __forceinline__ unsigned vo_rows(int lane, unsigned ld) { return (unsigned)(lane >> 3) * ld + (unsigned)(((lane & 7) ^ (lane >> 4)) * 16); }
__device__ __forceinline__ unsigned vo_tr(int lane, unsigned rowoff) { return rowoff + (unsigned)(((lane & 7) ^ (((lane >> 4) & 1) << 1)) * 16); }
__device__ __forceinline__ int tr_base(int lane, int c) {
    const int g = lane >> 4, q = (lane & 15) >> 2, p = lane & 3;
    const int f = ((q >> 1) & 1) | ((g & 1) << 1);
    return 128 * (8 * g + q) + ((((c ^ f) << 1) | (p >> 1)) * 16) + 8 * (p & 1);
}

__device__ __forceinline__ void stage_write(LAS char* lds, const f32x4 (&acc)[4][4], int wm, int wn, int lane) {
    const int fr = lane & 15, g = lane >> 4;
#pragma unroll
    for (int m = 0; m < 4; ++m)
#pragma unroll
        for (int n = 0; n < 4; ++n) {
            const int row = wm * 64 + m * 16 + fr;
            const int ch = (wn * 8 + 2 * n + (g >> 1)) ^ ((fr >> 1) & 7);
            u32x2 w;
            w.x = pk(acc[m][n][0], acc[m][n][1]);
            w.y = pk(acc[m][n][2], acc[m][n][3]);
            *(LAS u32x2*)(lds + row * 256 + ch * 16 + 8 * (g & 1)) = w;
        }
}
#define OUTAUX 0
__device__ __forceinline__ void stage_store(LAS char* lds, char* out, unsigned dst, int wid, int lane) {
    const rsrc_t ro = mk(out, 2048 * 2048 * 2);
#pragma unroll
    for (int i = 0; i < 4; ++i) {
        const int row = wid * 16 + i * 4 + (lane >> 4);
        const int ch = (lane & 15) ^ ((row >> 1) & 7);
        const u32x4 v = *(const LAS u32x4*)(lds + row * 256 + ch * 16);
        st128<OUTAUX>(v, ro, dst + (unsigned)row * 4096 + (lane & 15) * 16);
    }
}

__device__ __forceinline__ void chain_b(LAS char* lds, int g, int i16, int wid, int lane, const char* bx, const char* cr, const char* ci,
                                        const char* hr, const char* hi, char* out, char* ws) {
    const int part = i16 >> 3, dt = i16 & 7;
    const int hh = i16 & 1, et = i16 >> 1;
    char* fl = ws + WS_FLAG;
    constexpr int ST1 = 49152, ST2 = 32768;
    STAMP(0);
    if (wid >= 4) {
        const int w4 = wid - 4;
        u64 ep = 0;
        if (w4 == 0) ep = ld_dev(fl + FL_EB + g * 128);
        {
            const rsrc_t rc = mk(part ? ci : cr, 2048 * 2048 * 2), rx = mk(bx, 2048 * 1024 * 2);
            const unsigned va0 = vo_rows(lane, 4096), va1 = va0 ^ 64;
            const unsigned vb0 = vo_tr(lane, (lane >> 3) * 2048), vb1 = vb0 ^ 64;
            const unsigned sa = (unsigned)(g * 256 + w4 * 64) * 4096;
            const unsigned sb = (unsigned)((w4 & 1) * 32) * 2048 + (unsigned)(dt * 128 + (w4 >> 1) * 64) * 2;
            loader_loop<3, 12, 32>([&](int k, int slot) {
                LAS char* l = lds + slot * ST1;
#pragma unroll
                for (int j = 0; j < 4; ++j) dma<AUX_X>(rx, l + 32768 + (w4 * 4 + j) * 1024, (j & 1) ? vb1 : vb0, sb + (unsigned)(k * 64 + j * 8) * 2048);
#pragma unroll
                for (int j = 0; j < 8; ++j) dma<AUX_C>(rc, l + (w4 * 8 + j) * 1024, (j & 1) ? va1 : va0, sa + (unsigned)j * 8 * 4096 + (unsigned)k * 128);
            }, DBGP(4));
        }
        STAMP(1);
        BAR();
        if (w4 == 0) {
            const u64 tag = mix(ep);
            if (lane == 0) st_dev(fl + FL_U + g * 128 + i16 * 8, tag);
            poll(fl + FL_U + g * 128, 16, tag, lane);
            if (i16 == 0 && lane == 0) st_dev(fl + FL_EB + g * 128, tag);
        }
        STAMP(2);
        BAR();
        {
            const rsrc_t ru = mk(ws + WS_U, 8u << 20), rh0 = mk(hr, 1024 * 1024 * 2), rh1 = mk(hi, 1024 * 1024 * 2);
            const unsigned vl = lane * 16;
            const unsigned vb0 = vo_rows(lane, 2048), vb1 = vb0 ^ 64;
            const unsigned su = (unsigned)(g * 16 + hh * 8 + w4 * 2) * 65536;
            const unsigned sb = (unsigned)(et * 128 + w4 * 32) * 2048;
            loader_loop<4, 8, 32>([&](int k, int slot) {
                LAS char* l = lds + slot * ST2;
                const rsrc_t rh = k < 16 ? rh0 : rh1;
#pragma unroll
                for (int j = 0; j < 4; ++j) dma<AUX_H>(rh, l + 16384 + (w4 * 4 + j) * 1024, (j & 1) ? vb1 : vb0, sb + (unsigned)j * 8 * 2048 + (unsigned)(k & 15) * 128);
#pragma unroll
                for (int j = 0; j < 4; ++j) dma<AUX_I>(ru, l + (w4 * 4 + j) * 1024, vl, su + (unsigned)(j >> 1) * 65536 + (unsigned)(k * 2 + (j & 1)) * 1024);
            }, DBGP(10));
        }
        STAMP(3);
        BAR();
        stage_store(lds, out, (unsigned)(g * 256 + hh * 128) * 4096 + 2048 + et * 256, wid, lane);
    } else {
        const int wm = wid >> 1, wn = wid & 1, fr = lane & 15, fq = lane >> 4;
        {
            f32x4 acc[8][4];
            bf16x8 a0[8], b0[4];
            zero_acc(acc);
            const int ra0 = (wm * 128 + fr) * 128 + ((fq ^ (fr >> 1)) * 16), ra1 = ra0 ^ 64;
            int rb[4];
#pragma unroll
            for (int c = 0; c < 4; ++c) rb[c] = 32768 + wn * 8192 + tr_base(lane, c);
            auto rdA = [&](int m, int kh, int so) { return ld128(lds + so + (kh ? ra1 : ra0) + m * 2048); };
            auto rdB = [&](int n, int kh, int t, int so) { return ldtr(lds + so + rb[n] + kh * 4096 + t * 512); };
            unsigned sink = 0;
            const u32x4 tc = srd(part ? ci : cr, 2048 * 2048 * 2), tx = srd(bx, 2048 * 1024 * 2);
            const unsigned vtc = (unsigned)(g * 256 + dt * 32 + (lane >> 1)) * 4096 + (lane & 1) * 64;
            const unsigned vtx = (unsigned)(((g & 1) * 2 + part) * 16 + (lane >> 2)) * 2048 + dt * 256 + (lane & 3) * 64;
            auto pf = [&](int k) {
                if (TD && k < 32) {
                    if (wid == 0) touch<(AUX_C & 2) != 0>(sink, vtc, tc, (unsigned)k * 128);
                    if (wid == 1) touch<(AUX_X & 2) != 0>(sink, vtx, tx, (unsigned)k * 131072);
                }
            };
            if (TD) for (int k = 3; k < TD; ++k) pf(k);
            BAR();
            STAMPC(4);
            first_frags(a0, b0, rdA, rdB);
            int slot = 0;
            compute_loop<8, 4, 4, 1, 32, 3, ST1>(acc, a0, b0, slot, rdA, rdB, [&](int k) { pf(k + TD); });
            STAMPC(5);
            touch_done(sink);
            const rsrc_t ru = mk(ws + WS_U, 8u << 20);
            const unsigned sg = part ? 0x80008000u : 0u;
            const unsigned so = (unsigned)((g * 16 + wm * 8) * 64 + part * 32 + dt * 4 + wn * 2) * 1024;
#pragma unroll
            for (int m = 0; m < 8; ++m)
#pragma unroll
                for (int np = 0; np < 2; ++np) {
                    u32x4 v = pk8(acc[m][2 * np], acc[m][2 * np + 1]);
                    v.x ^= sg; v.y ^= sg; v.z ^= sg; v.w ^= sg;
                    st128<AUX_DEV>(v, ru, lane * 16 + so + (unsigned)(m * 64 + np) * 1024);
                }
            WAIT_V(0);
            STAMP(6);
        }
        unsigned sink = 0;
        const u32x4 th0 = srd(hr, 1024 * 1024 * 2), th1 = srd(hi, 1024 * 1024 * 2), tu = srd(ws + WS_U, 8u << 20);
        const unsigned vth = (unsigned)(et * 128 + ((g & 1) * 2 + hh) * 32 + (lane >> 1)) * 2048 + (lane & 1) * 64;
        const unsigned vtu = (unsigned)(g * 16 + hh * 8 + et) * 65536 + (lane & 31) * 64;
        auto pfh = [&](int k) { if (TD && k < 32 && wid == 0) touch<(AUX_H & 2) != 0>(sink, vth, k < 16 ? th0 : th1, (unsigned)(k & 15) * 128); };
        auto pfu = [&](int k) { if (TD && k < 32 && wid == 1) touch<0>(sink, vtu, tu, (unsigned)k * 2048); };
        if (TD) for (int k = 0; k < TD; ++k) pfh(k);
        BAR();
        BAR();
        if (TD) for (int k = 4; k < TD; ++k) pfu(k);
        {
            f32x4 acc[4][4];
            bf16x8 a0[4], b0[4];
            zero_acc(acc);
            const int ra = wm * 8192 + lane * 16;
            int rb[4];
#pragma unroll
            for (int c = 0; c < 4; ++c) rb[c] = 16384 + (wn * 64 + fr) * 128 + (((c * 2 + (fq >> 1)) ^ (fr >> 1)) * 16) + 8 * (fq & 1);
            auto rdA = [&](int m, int kh, int so) { return ld128(lds + so + ra + m * 2048 + kh * 1024); };
            auto rdB = [&](int n, int kh, int t, int so) { return ld64(lds + so + rb[kh * 2 + t] + n * 2048); };
            BAR();
            STAMPC(7);
            first_frags(a0, b0, rdA, rdB);
            int slot = 0;
            compute_loop<4, 4, 4, 2, 32, 4, ST2>(acc, a0, b0, slot, rdA, rdB, [&](int k) { pfh(k + TD); pfu(k + TD); });
            STAMPC(8);
            touch_done(sink);
            stage_write(lds, acc, wm, wn, lane);
            WAIT_L(0);
        }
        BAR();
        stage_store(lds, out, (unsigned)(g * 256 + hh * 128) * 4096 + 2048 + et * 256, wid, lane);
    }
    STAMP(9);
}

template <bool FIRST, int PAR>
__device__ __forceinline__ u32x4 attn_strip(LAS char* img, int s, bf16x8 qf, int lane) {
    constexpr float C = 0.17677669529663687f * 1.4426950408889634f;
    constexpr int JL = PAR, JD = PAR + 16;
    LAS char* p = img + (s & ~1) * 4096;
    const int r = lane & 15, g4 = (lane >> 4) * 4;
    f32x4 sc[18];
#pragma unroll
    for (int j = JL; j <= JD; ++j) sc[j] = MFMA(ld128(p + j * 4096), qf, ((f32x4){0.f, 0.f, 0.f, 0.f}));
    const float ninf = -__builtin_inff();
    f32x4 mg;
#pragma unroll
    for (int e = 0; e < 4; ++e) mg[e] = (g4 + e > r) ? (FIRST ? ninf : sc[JL][e]) : sc[JD][e];
    if (FIRST) {
#pragma unroll
        for (int j = JL + 1; j < JD; ++j) {
            const bool bad = (s & ~1) + j < 16;
#pragma unroll
            for (int e = 0; e < 4; ++e) sc[j][e] = bad ? ninf : sc[j][e];
        }
    }
    float mx = fmaxf(fmaxf(mg[0], mg[1]), fmaxf(mg[2], mg[3]));
#pragma unroll
    for (int j = JL + 1; j < JD; ++j) {
        mx = fmaxf(fmaxf(mx, sc[j][0]), sc[j][1]);
        mx = fmaxf(fmaxf(mx, sc[j][2]), sc[j][3]);
    }
    {
        auto a = __builtin_amdgcn_permlane16_swap(__float_as_uint(mx), __float_as_uint(mx), false, false);
        mx = fmaxf(__uint_as_float(a[0]), __uint_as_float(a[1]));
        auto b = __builtin_amdgcn_permlane32_swap(__float_as_uint(mx), __float_as_uint(mx), false, false);
        mx = fmaxf(__uint_as_float(b[0]), __uint_as_float(b[1]));
    }
    const float mc = -mx * C;
#pragma unroll
    for (int e = 0; e < 4; ++e) mg[e] = __builtin_amdgcn_exp2f(fmaf(mg[e], C, mc));
#pragma unroll
    for (int j = JL + 1; j < JD; ++j)
#pragma unroll
        for (int e = 0; e < 4; ++e) sc[j][e] = __builtin_amdgcn_exp2f(fmaf(sc[j][e], C, mc));
#pragma unroll
    for (int e = 0; e < 4; ++e) {
        const bool lo = g4 + e > r;
        sc[JL][e] = lo ? mg[e] : 0.f;
        sc[JD][e] = lo ? 0.f : mg[e];
    }
    sc[PAR ? 0 : 17] = (f32x4){0.f, 0.f, 0.f, 0.f};
    f32x4 o0 = {0.f, 0.f, 0.f, 0.f}, o1 = o0, sum = o0;
    const bf16x8 ones = {0x3F80, 0x3F80, 0x3F80, 0x3F80, 0x3F80, 0x3F80, 0x3F80, 0x3F80};
#pragma unroll
    for (int j2 = 0; j2 < 9; ++j2) {
        const bf16x8 pf = __builtin_bit_cast(bf16x8, pk8(sc[2 * j2], sc[2 * j2 + 1]));
        o0 = MFMA(ld128(p + (2 * j2) * 4096 + 2048), pf, o0);
        o1 = MFMA(ld128(p + (2 * j2 + 1) * 4096 + 2048), pf, o1);
        sum = MFMA(ones, pf, sum);
    }
    const float inv = 1.0f / sum[0];
    o0 *= inv;
    o1 *= inv;
    return pk8(o0, o1);
}

template <bool FIRST>
__device__ __forceinline__ void attention(LAS char* lds, int r, int hp, int wid, int lane, char* ws) {
    const int h = wid & 1, sp = wid >> 1;
    const int head = hp * 2 + h;
    const rsrc_t rq = mk(ws + WS_Q, 4u << 20), ro = mk(ws + WS_O, 4u << 20);
    LAS char* img = lds + 32768 + h * 1024 + lane * 16;
    bf16x8 qf[4];
#pragma unroll
    for (int i = 0; i < 4; ++i) {
        const int s = (i < 2 ? 8 : 0) + sp * 2 + (i & 1);
        qf[i] = __builtin_bit_cast(bf16x8, __builtin_amdgcn_raw_buffer_load_b128(rq, lane * 16, (unsigned)(head * 128 + r * 16 + s) * 1024, AUX_DEV));
    }
    WAIT_V(0);
    BAR();
#pragma unroll
    for (int i = 0; i < 2; ++i) {
        const int s0 = (i == 0 ? 8 : 0) + sp * 2;
        const u32x4 v0 = attn_strip<FIRST, 0>(img, s0, qf[2 * i], lane);
        st128<AUX_DEV>(v0, ro, lane * 16 + (unsigned)((r * 16 + s0) * 32 + head) * 1024);
        const u32x4 v1 = attn_strip<FIRST, 1>(img, s0 + 1, qf[2 * i + 1], lane);
        st128<AUX_DEV>(v1, ro, lane * 16 + (unsigned)((r * 16 + s0 + 1) * 32 + head) * 1024);
    }
    WAIT_V(0);
}

__device__ __forceinline__ void chain_a(LAS char* lds, int r, int hp, int wid, int lane, const char* ax, const char* wqkv, const char* wo,
                                        char* out, char* ws) {
    char* fl = ws + WS_FLAG;
    constexpr int ST1 = 40960, ST3 = 32768;
    u64 ep0 = 0, ep1 = 0;
    STAMP(0);
    if (wid >= 4) {
        const int w4 = wid - 4;
        if (w4 == 0) {
            ep0 = ld_dev(fl + FL_EA + r * 128);
            ep1 = ld_dev(fl + FL_EA + ((r + 1) & 7) * 128);
        }
        const rsrc_t rx = mk(ax, 2048 * 1024 * 2), rw = mk(wqkv, 1024 * 3072 * 2);
        const unsigned va0 = vo_rows(lane, 2048), va1 = va0 ^ 64;
        const unsigned vb0 = vo_tr(lane, (lane >> 3) * 6144), vb1 = vb0 ^ 64;
        const unsigned sa = (unsigned)(r * 256 + w4 * 32) * 2048;
        unsigned sb[6];
#pragma unroll
        for (int j = 0; j < 6; ++j) {
            const int i = w4 * 6 + j;
            sb[j] = (unsigned)((i & 7) * 8) * 6144 + (unsigned)((i >> 3) * 1024 + hp * 64) * 2;
        }
        loader_loop<3, 10, 32>([&](int k, int slot) {
            LAS char* l = lds + slot * ST1;
            const unsigned kk = k & 15, sub = (k >> 4) ^ 1;
#pragma unroll
            for (int j = 0; j < 6; ++j) dma<AUX_W>(rw, l + 16384 + (w4 * 6 + j) * 1024, (j & 1) ? vb1 : vb0, sb[j] + kk * 64 * 6144);
#pragma unroll
            for (int j = 0; j < 4; ++j) dma<AUX_AX>(rx, l + (w4 * 4 + j) * 1024, (j & 1) ? va1 : va0, sa + (sub * 128 + j * 8) * 2048 + kk * 128);
        }, DBGP(4));
        STAMP(1);
    } else {
        const int wm = wid >> 1, h = wid & 1, fr = lane & 15, fq = lane >> 4;
        f32x4 acc[4][6];
        bf16x8 a0[4], b0[6];
        zero_acc(acc);
        const int ra0 = (wm * 64 + fr) * 128 + ((fq ^ (fr >> 1)) * 16), ra1 = ra0 ^ 64;
        const int rb0 = 16384 + tr_base(lane, 2 * h), rb1 = 16384 + tr_base(lane, 2 * h + 1);
        auto rdA = [&](int m, int kh, int so) { return ld128(lds + so + (kh ? ra1 : ra0) + m * 2048); };
        auto rdB = [&](int n, int kh, int t, int so) { return ldtr(lds + so + ((n & 1) ? rb1 : rb0) + (n >> 1) * 8192 + kh * 4096 + t * 512); };
        const rsrc_t rq = mk(ws + WS_Q, 4u << 20), rkv = mk(ws + WS_KV, 8u << 20);
        unsigned sink = 0;
        const u32x4 tx = srd(ax, 2048 * 1024 * 2), tw = srd(wqkv, 1024 * 3072 * 2);
        const unsigned vtx = (unsigned)(r * 256 + (hp & 3) * 32 + (lane >> 1)) * 2048 + (lane & 1) * 64;
        const unsigned vtw = (unsigned)(r * 8 + ((lane >> 1) & 7)) * 6144 + (unsigned)(((lane >> 4) % 3) * 1024 + hp * 64) * 2 + (lane & 1) * 64;
        auto pf = [&](int k) {
            if (TDA && k < 32) {
                if (wid == 0) touch<(AUX_AX & 2) != 0>(sink, vtx, tx, (unsigned)(((k >> 4) ^ 1) * 128) * 2048 + (unsigned)(k & 15) * 128);
                if (wid == 1 && k < 16) touch<(AUX_W & 2) != 0>(sink, vtw, tw, (unsigned)k * 64 * 6144);
            }
        };
        if (TDA) for (int k = 3; k < TDA; ++k) pf(k);
        BAR();
        STAMPC(4);
        first_frags(a0, b0, rdA, rdB);
        int slot = 0;
#pragma unroll 1
        for (int it = 0; it < 2; ++it) {
            compute_loop<4, 6, 4, 1, 16, 3, ST1>(acc, a0, b0, slot, rdA, rdB, [&](int k) { pf(it * 16 + k + TDA); });
            const int t0 = r * 16 + (it ^ 1) * 8 + wm * 4;
            const unsigned sq = (unsigned)((hp * 2 + h) * 128 + t0) * 1024;
            const unsigned sk = (unsigned)(hp * 128 + t0) * 4096 + (unsigned)h * 1024;
#pragma unroll
            for (int m = 0; m < 4; ++m) {
                st128<AUX_DEV>(pk8(acc[m][2], acc[m][3]), rkv, lane * 16 + sk + m * 4096);
                if (m & 1) {
                    st128<AUX_DEV>(pk8(acc[m - 1][4], acc[m][4]), rkv, lane * 16 + sk + (m - 1) * 4096 + 2048);
                    st128<AUX_DEV>(pk8(acc[m - 1][5], acc[m][5]), rkv, lane * 16 + sk + m * 4096 + 2048);
                }
                st128<AUX_DEV>(pk8(acc[m][0], acc[m][1]), rq, lane * 16 + sq + m * 1024);
            }
            zero_acc(acc);
        }
        STAMPC(5);
        touch_done(sink);
        STAMP(6);
    }
    BAR();
    {
        const rsrc_t rkv = mk(ws + WS_KV, 8u << 20);
        const unsigned vl = lane * 16;
        const unsigned own = (unsigned)(hp * 128 + r * 16) * 4096;
        if (wid == 4) {
            if (lane == 0 && r < 7) st_dev(fl + FL_KV + r * 128 + hp * 8, mix(ep1));
        }
#pragma unroll
        for (int j = 0; j < 8; ++j) dma<AUX_DEV>(rkv, lds + 32768 + 65536 + (wid * 8 + j) * 1024, vl, own + (unsigned)(wid * 8 + j) * 1024);
        if (r > 0) {
            if (wid == 4) poll(fl + FL_KV + (r - 1) * 128 + hp * 8, 1, mix(ep0), lane);
            BAR();
#pragma unroll
            for (int j = 0; j < 8; ++j) dma<AUX_DEV>(rkv, lds + 32768 + (wid * 8 + j) * 1024, vl, own - 65536 + (unsigned)(wid * 8 + j) * 1024);
            STAMP(2);
            attention<false>(lds, r, hp, wid, lane, ws);
        } else {
#pragma unroll
            for (int j = 0; j < 8; ++j) *(LAS u32x4*)(lds + 32768 + (wid * 8 + j) * 1024 + vl) = (u32x4){0u, 0u, 0u, 0u};
            WAIT_L(0);
            STAMP(2);
            attention<true>(lds, r, hp, wid, lane, ws);
        }
    }
    STAMP(3);
    BAR();
    const int hh = hp & 1, ct = hp >> 1;
    const unsigned dst = (unsigned)(r * 256 + hh * 128) * 4096 + ct * 256;
    if (wid >= 4) {
        const int w4 = wid - 4;
        if (w4 == 0) {
            const u64 tag = mix(ep0);
            if (lane == 0) st_dev(fl + FL_O + r * 128 + hp * 8, tag);
            poll(fl + FL_O + r * 128, 16, tag, lane);
            if (hp == 0 && lane == 0) st_dev(fl + FL_EA + r * 128, tag);
        }
        STAMP(7);
        BAR();
        const rsrc_t ro = mk(ws + WS_O, 4u << 20), rw = mk(wo, 1024 * 1024 * 2);
        const unsigned vl = lane * 16;
        const unsigned vb0 = vo_tr(lane, (unsigned)(16 * (lane >> 5) + ((lane >> 3) & 3)) * 2048), vb1 = vb0 ^ 64;
        const unsigned so = (unsigned)(r * 16 + hh * 8 + w4 * 2) * 32768;
        const unsigned sb = (unsigned)((w4 & 1) * 32) * 2048 + (unsigned)(ct * 128 + (w4 >> 1) * 64) * 2;
        loader_loop<4, 8, 16>([&](int k, int slot) {
            LAS char* l = lds + slot * ST3;
#pragma unroll
            for (int j = 0; j < 4; ++j) dma<AUX_WO>(rw, l + 16384 + (w4 * 4 + j) * 1024, (j & 1) ? vb1 : vb0, sb + (unsigned)(k * 64 + j * 4) * 2048);
#pragma unroll
            for (int j = 0; j < 4; ++j) dma<AUX_I>(ro, l + (w4 * 4 + j) * 1024, vl, so + (unsigned)(j >> 1) * 32768 + (unsigned)(k * 2 + (j & 1)) * 1024);
        }, DBGP(10));
        STAMP(8);
        BAR();
        stage_store(lds, out, dst, wid, lane);
    } else {
        const int wm = wid >> 1, wn = wid & 1;
        f32x4 acc[4][4];
        bf16x8 a0[4], b0[4];
        zero_acc(acc);
        const int ra = wm * 8192 + lane * 16;
        int rb[4];
#pragma unroll
        for (int c = 0; c < 4; ++c) rb[c] = 16384 + wn * 8192 + tr_base(lane, c);
        auto rdA = [&](int m, int kh, int so) { return ld128(lds + so + ra + m * 2048 + kh * 1024); };
        auto rdB = [&](int n, int kh, int t, int so) { return ldtr(lds + so + rb[n] + kh * 4096 + t * 512); };
        unsigned sink = 0;
        const u32x4 tw = srd(wo, 1024 * 1024 * 2), to = srd(ws + WS_O, 4u << 20);
        const unsigned vtw = (unsigned)((r * 2 + hh) * 4 + ((lane >> 2) & 3)) * 2048 + ct * 256 + (lane & 3) * 64;
        const unsigned vto = (unsigned)(r * 16 + hh * 8 + (ct & 1) * 4 + (lane >> 4)) * 32768 + (lane & 15) * 64;
        auto pfw = [&](int k) { if (TDA && k < 16 && wid == 0) touch<(AUX_WO & 2) != 0>(sink, vtw, tw, (unsigned)k * 64 * 2048); };
        auto pfo = [&](int k) { if (TDA && k < 16 && wid >= 1 && wid <= 2) touch<0>(sink, vto, to, (unsigned)k * 2048 + (wid - 1) * 1024); };
        if (TDA) for (int k = 0; k < TDA; ++k) pfw(k);
        BAR();
        BAR();
        if (TDA) for (int k = 4; k < TDA; ++k) pfo(k);
        STAMPC(10);
        first_frags(a0, b0, rdA, rdB);
        int slot = 0;
        compute_loop<4, 4, 4, 2, 16, 4, ST3>(acc, a0, b0, slot, rdA, rdB, [&](int k) { pfw(k + TDA); pfo(k + TDA); });
        STAMPC(11);
        touch_done(sink);
        stage_write(lds, acc, wm, wn, lane);
        WAIT_L(0);
        BAR();
        stage_store(lds, out, dst, wid, lane);
    }
    STAMP(9);
}

__global__ __launch_bounds__(512, 2) void fused_kernel(const char* ax, const char* wqkv, const char* wo, const char* bx, const char* cr,
                                                       const char* ci, const char* hr, const char* hi, char* out, char* ws) {
    extern __shared__ __attribute__((aligned(16))) char smem[];
    LAS char* lds = (LAS char*)smem;
    const int wid = __builtin_amdgcn_readfirstlane(threadIdx.x >> 6), lane = threadIdx.x & 63;
    const int x = blockIdx.x & 7, idx = blockIdx.x >> 3;
    const int j = x >> 1;
    if (x & 1) chain_b(lds, 2 * j + (idx >> 4), idx & 15, wid, lane, bx, cr, ci, hr, hi, out, ws);
    else if (MAPA == 1) chain_a(lds, 2 * j + (idx >> 4), idx & 15, wid, lane, ax, wqkv, wo, out, ws);
    else chain_a(lds, idx >> 2, 4 * j + (idx & 3), wid, lane, ax, wqkv, wo, out, ws);
}

}

extern "C" void kernel_launch(void* const* d_in, const int* in_sizes, int n_in, void* d_out, int out_size, void* d_ws, size_t ws_size,
                              hipStream_t stream) {
    (void)in_sizes; (void)n_in; (void)out_size; (void)ws_size;
    (void)hipFuncSetAttribute((const void*)fused_kernel, hipFuncAttributeMaxDynamicSharedMemorySize, LDS_BYTES);
    fused_kernel<<<256, 512, LDS_BYTES, stream>>>((const char*)d_in[0], (const char*)d_in[1], (const char*)d_in[2], (const char*)d_in[3],
                                                  (const char*)d_in[4], (const char*)d_in[5], (const char*)d_in[6], (const char*)d_in[7],
                                                  (char*)d_out, (char*)d_ws);
}
```

```cpp
#include <hip/hip_runtime.h>
#include <stdint.h>

#define LAS __attribute__((address_space(3)))
typedef short bf16x8 __attribute__((ext_vector_type(8)));
typedef short s16x4 __attribute__((ext_vector_type(4)));
typedef float f32x2 __attribute__((ext_vector_type(2)));
typedef float f32x4 __attribute__((ext_vector_type(4)));
typedef __bf16 bfx2 __attribute__((ext_vector_type(2)));
typedef unsigned u32x4 __attribute__((ext_vector_type(4)));
typedef unsigned u32x2 __attribute__((ext_vector_type(2)));
typedef unsigned long long u64;
typedef __amdgpu_buffer_rsrc_t rsrc_t;

#define WAIT_V(n) asm volatile("s_waitcnt vmcnt(%0)" ::"n"(n) : "memory")
#define WAIT_L(n) asm volatile("s_waitcnt lgkmcnt(%0)" ::"n"(n) : "memory")
#define BAR() do { asm volatile("" ::: "memory"); __builtin_amdgcn_s_barrier(); asm volatile("" ::: "memory"); } while (0)
#define SCHED() __builtin_amdgcn_sched_barrier(0)
#define MFMA(a, b, c) __builtin_amdgcn_mfma_f32_16x16x32_bf16(a, b, c, 0, 0, 0)

#define STAMPS 0
#define MAPA 2
#define STAMP(i) do { } while (0)
#define STAMPC(i) do { } while (0)
#define DBGP(i) nullptr

namespace {

constexpr int AUX_DEV = 16;
constexpr unsigned WS_U = 0;
constexpr unsigned WS_KV = 8u << 20;
constexpr unsigned WS_Q = 16u << 20;
constexpr unsigned WS_O = 20u << 20;
constexpr unsigned WS_FLAG = 24u << 20;
constexpr unsigned WS_STAMP = 25u << 20;
constexpr unsigned WS_BYTES = 26u << 20;
constexpr unsigned FL_EB = 0, FL_EA = 1024, FL_U = 2048, FL_O = 3072, FL_KV = 4096;
constexpr int LDS_BYTES = 160 * 1024;
constexpr int SPIN_MAX = 100000;

__device__ __forceinline__ unsigned pk(float a, float b) {
    f32x2 v = {a, b};
    return __builtin_bit_cast(unsigned, __builtin_convertvector(v, bfx2));
}
__device__ __forceinline__ u32x4 pk8(const f32x4& a, const f32x4& b) {
    u32x4 r;
    r.x = pk(a[0], a[1]); r.y = pk(a[2], a[3]); r.z = pk(b[0], b[1]); r.w = pk(b[2], b[3]);
    return r;
}
__device__ __forceinline__ rsrc_t mk(const void* p, unsigned bytes) { return __builtin_amdgcn_make_buffer_rsrc((void*)p, 0, (int)bytes, 0x00020000); }
template <int AUX>
__device__ __forceinline__ void dma(rsrc_t r, LAS char* l, unsigned vo, unsigned so) {
    __builtin_amdgcn_raw_ptr_buffer_load_lds(r, (LAS void*)l, 16, (int)vo, (int)so, 0, AUX);
}
template <int AUX>
__device__ __forceinline__ void st128(u32x4 v, rsrc_t r, unsigned vo) { __builtin_amdgcn_raw_buffer_store_b128(v, r, (int)vo, 0, AUX); }
__device__ __forceinline__ bf16x8 ld128(LAS char* p) { return *(const LAS bf16x8*)p; }
__device__ __forceinline__ s16x4 ld64(LAS char* p) { return *(const LAS s16x4*)p; }
__device__ __forceinline__ s16x4 ldtr(LAS char* p) { return __builtin_amdgcn_ds_read_tr16_b64_v4i16((LAS s16x4*)p); }
__device__ __forceinline__ bf16x8 cat(s16x4 a, s16x4 b) { return __builtin_shufflevector(a, b, 0, 1, 2, 3, 4, 5, 6, 7); }

#define TD 0
#define TDA TD
#define AUX_C 0
#define AUX_AX 0
#define AUX_X 0
#define AUX_H 0
#define AUX_W 0
#define AUX_WO 0
#define AUX_I 16
#define ROT1 (i16 * 2 + g)
#define ROT2 (g * 4)
#define ROTQ (r * 2 + hp)
#define ROTP (r * 2)
#define RG1 0
#define RG2 0
#define RGQ 0
#define RGP 0
__device__ __forceinline__ u32x4 srd(const void* p, unsigned bytes) {
    const u64 a = (u64)p;
    u32x4 r;
    r.x = __builtin_amdgcn_readfirstlane((unsigned)a);
    r.y = __builtin_amdgcn_readfirstlane((unsigned)(a >> 32));
    r.z = bytes;
    r.w = 0x00020000u;
    return r;
}
template <int NT>
__device__ __forceinline__ void touch(unsigned& sink, unsigned vo, const u32x4& r, unsigned so) {
    if (NT) asm volatile("buffer_load_dword %0, %1, %2, %3 offen nt" : "+v"(sink) : "v"(vo), "s"(r), "s"(so));
    else asm volatile("buffer_load_dword %0, %1, %2, %3 offen" : "+v"(sink) : "v"(vo), "s"(r), "s"(so));
}
__device__ __forceinline__ void touch_done(unsigned& sink) { asm volatile("s_waitcnt vmcnt(0)" : "+v"(sink)::"memory"); }

__device__ __forceinline__ u64 mix(u64 z) {
    z += 0x9E3779B97F4A7C15ull;
    z = (z ^ (z >> 30)) * 0xBF58476D1CE4E5B9ull;
    z = (z ^ (z >> 27)) * 0x94D049BB133111EBull;
    return z ^ (z >> 31);
}
__device__ __forceinline__ u64 ld_dev(const char* p) { return __hip_atomic_load((const u64*)p, __ATOMIC_RELAXED, __HIP_MEMORY_SCOPE_AGENT); }
__device__ __forceinline__ void st_dev(char* p, u64 v) { __hip_atomic_store((u64*)p, v, __ATOMIC_RELAXED, __HIP_MEMORY_SCOPE_AGENT); }
__device__ __forceinline__ void poll(const char* base, int n, u64 tag, int lane) {
    const char* p = base + (lane & (n - 1)) * 8;
    for (int it = 0; it < SPIN_MAX; ++it) {
        const u64 v = ld_dev(p);
        if (__all(v == tag)) break;
        __builtin_amdgcn_s_sleep(2);
    }
}

template <int S, int P, int NK, class ISSUE>
__device__ __forceinline__ void loader_loop(ISSUE issue, u64* dbg = nullptr) {
#pragma unroll
    for (int s = 0; s < S; ++s) issue(s, s);
    WAIT_V((S - 1) * P);
    BAR();
    int slot = 0;
    for (int k = 0; k < NK - S; ++k) {
        WAIT_V((S - 2) * P);
        BAR();
        issue(k + S, slot);
        slot = slot == S - 1 ? 0 : slot + 1;
    }
#pragma unroll
    for (int t = 0; t < S; ++t) {
        WAIT_V((S - 2 - t > 0 ? S - 2 - t : 0) * P);
        BAR();
    }
}

template <int AUX>
__device__ __forceinline__ u32x4 ld128g(rsrc_t r, unsigned vo, unsigned so) { return __builtin_amdgcn_raw_buffer_load_b128(r, (int)vo, (int)so, AUX); }
template <int P, int D, int J0, int J1, class LOAD>
__device__ __forceinline__ void reg_prologue(u32x4 (&buf)[D][P], LOAD load) {
#pragma unroll
    for (int s = 0; s < D; ++s)
#pragma unroll
        for (int j = J0; j < J1; ++j) buf[s][j] = load(s, j);
}
template <int P, int D, int NK, int STAGE, class LOAD, class DST>
__device__ __forceinline__ void reg_loop(u32x4 (&buf)[D][P], LAS char* lds, LOAD load, DST dst, u64* dbg = nullptr) {
    static_assert(D % 2 == 0 && NK % D == 0, "ring shape");
    for (int c = 0; c < NK / D - 1; ++c) {
#pragma unroll
        for (int d = 0; d < D; ++d) {
#pragma unroll
            for (int j = 0; j < P; ++j) {
                *(LAS u32x4*)(lds + (d & 1) * STAGE + dst(j)) = buf[d][j];
                buf[d][j] = load(c * D + d + D, j);
                SCHED();
            }
            WAIT_L(0);
            BAR();
        }
    }
#pragma unroll
    for (int d = 0; d < D; ++d) {
#pragma unroll
        for (int j = 0; j < P; ++j) {
            *(LAS u32x4*)(lds + (d & 1) * STAGE + dst(j)) = buf[d][j];
            SCHED();
        }
        WAIT_L(0);
        BAR();
    }
    BAR();
}

template <int MT, int NT, int SWAP0, int RPG, class FA, class FB>
__device__ __forceinline__ void half_step(f32x4 (&acc)[MT][NT], const bf16x8 (&ac)[MT], const bf16x8 (&bc)[NT],
                                          bf16x8 (&an)[MT], bf16x8 (&bn)[NT], FA rdA, FB rdB) {
    s16x4 lo[NT];
    constexpr int GB = (2 * NT + RPG - 1) / RPG;
#pragma unroll
    for (int m = 0; m < MT; ++m) {
#pragma unroll
        for (int n = 0; n < NT; ++n) {
            const int i = m * NT + n;
            acc[m][n] = (n >= SWAP0) ? MFMA(ac[m], bc[n], acc[m][n]) : MFMA(bc[n], ac[m], acc[m][n]);
            SCHED();
            if (i < GB) {
#pragma unroll
                for (int j = 0; j < RPG; ++j) {
                    const int o = i * RPG + j;
                    if (o < 2 * NT) {
                        if ((o & 1) == 0) lo[o >> 1] = rdB(o >> 1, 0);
                        else bn[o >> 1] = cat(lo[o >> 1], rdB(o >> 1, 1));
                    }
                }
                SCHED();
            } else if (i - GB < MT) {
                an[i - GB] = rdA(i - GB);
                SCHED();
            }
        }
    }
}

template <int MT, int NT, int SWAP0, int RPG, int NK, int S, int STAGE, class FA, class FB, class PF>
__device__ __forceinline__ void compute_loop(f32x4 (&acc)[MT][NT], bf16x8 (&a0)[MT], bf16x8 (&b0)[NT], int& slot, FA rdA, FB rdB, PF pf) {
    bf16x8 a1[MT], b1[NT];
    for (int k = 0; k < NK; ++k) {
        const int so = slot * STAGE;
        const int nslot = slot == S - 1 ? 0 : slot + 1;
        const int sn = nslot * STAGE;
        SCHED();
        half_step<MT, NT, SWAP0, RPG>(acc, a0, b0, a1, b1, [&](int m) { return rdA(m, 1, so); }, [&](int n, int t) { return rdB(n, 1, t, so); });
        WAIT_L(0);
        BAR();
        pf(k);
        SCHED();
        half_step<MT, NT, SWAP0, RPG>(acc, a1, b1, a0, b0, [&](int m) { return rdA(m, 0, sn); }, [&](int n, int t) { return rdB(n, 0, t, sn); });
        slot = nslot;
    }
}
template <int MT, int NT, class FA, class FB>
__device__ __forceinline__ void first_frags(bf16x8 (&a0)[MT], bf16x8 (&b0)[NT], FA rdA, FB rdB) {
#pragma unroll
    for (int n = 0; n < NT; ++n) b0[n] = cat(rdB(n, 0, 0, 0), rdB(n, 0, 1, 0));
#pragma unroll
    for (int m = 0; m < MT; ++m) a0[m] = rdA(m, 0, 0);
}
template <int MT, int NT>
__device__ __forceinline__ void zero_acc(f32x4 (&acc)[MT][NT]) {
#pragma unroll
    for (int m = 0; m < MT; ++m)
#pragma unroll
        for (int n = 0; n < NT; ++n) acc[m][n] = (f32x4){0.f, 0.f, 0.f, 0.f};
}

__device__ __forceinline__ unsigned vo_rows(int lane, unsigned ld) { return (unsigned)(lane >> 3) * ld + (unsigned)(((lane & 7) ^ (lane >> 4)) * 16); }
__device__ __forceinline__ unsigned vo_tr(int lane, unsigned rowoff) { return rowoff + (unsigned)(((lane & 7) ^ (((lane >> 4) & 1) << 1)) * 16); }
__device__ __forceinline__ int tr_base(int lane, int c) {
    const int g = lane >> 4, q = (lane & 15) >> 2, p = lane & 3;
    const int f = ((q >> 1) & 1) | ((g & 1) << 1);
    return 128 * (8 * g + q) + ((((c ^ f) << 1) | (p >> 1)) * 16) + 8 * (p & 1);
}

__device__ __forceinline__ void stage_write(LAS char* lds, const f32x4 (&acc)[4][4], int wm, int wn, int lane) {
    const int fr = lane & 15, g = lane >> 4;
#pragma unroll
    for (int m = 0; m < 4; ++m)
#pragma unroll
        for (int n = 0; n < 4; ++n) {
            const int row = wm * 64 + m * 16 + fr;
            const int ch = (wn * 8 + 2 * n + (g >> 1)) ^ ((fr >> 1) & 7);
            u32x2 w;
            w.x = pk(acc[m][n][0], acc[m][n][1]);
            w.y = pk(acc[m][n][2], acc[m][n][3]);
            *(LAS u32x2*)(lds + row * 256 + ch * 16 + 8 * (g & 1)) = w;
        }
}
#define OUTAUX 0
__device__ __forceinline__ void stage_store(LAS char* lds, char* out, unsigned dst, int wid, int lane) {
    const rsrc_t ro = mk(out, 2048 * 2048 * 2);
#pragma unroll
    for (int i = 0; i < 4; ++i) {
        const int row = wid * 16 + i * 4 + (lane >> 4);
        const int ch = (lane & 15) ^ ((row >> 1) & 7);
        const u32x4 v = *(const LAS u32x4*)(lds + row * 256 + ch * 16);
        st128<OUTAUX>(v, ro, dst + (unsigned)row * 4096 + (lane & 15) * 16);
    }
}

__device__ __forceinline__ void chain_b(LAS char* lds, int g, int i16, int wid, int lane, const char* bx, const char* cr, const char* ci,
                                        const char* hr, const char* hi, char* out, char* ws) {
    const int part = i16 >> 3, dt = i16 & 7;
    const int hh = i16 & 1, et = i16 >> 1;
    char* fl = ws + WS_FLAG;
    constexpr int ST1 = 49152, ST2 = 32768;
    STAMP(0);
    if (wid >= 4) {
        const int w4 = wid - 4;
        u64 ep = 0;
        if (w4 == 0) ep = ld_dev(fl + FL_EB + g * 128);
        {
            const rsrc_t rc = mk(part ? ci : cr, 2048 * 2048 * 2), rx = mk(bx, 2048 * 1024 * 2);
            const unsigned va0 = vo_rows(lane, 4096), va1 = va0 ^ 64;
            const unsigned vb0 = vo_tr(lane, (lane >> 3) * 2048), vb1 = vb0 ^ 64;
            const unsigned sa = (unsigned)(g * 256 + w4 * 64) * 4096;
            const unsigned sb = (unsigned)((w4 & 1) * 32) * 2048 + (unsigned)(dt * 128 + (w4 >> 1) * 64) * 2;
            const int rot = ROT1;
            loader_loop<3, 12, 32>([&](int k0, int slot) {
                const int k = (k0 + rot) & 31;
                LAS char* l = lds + slot * ST1;
#pragma unroll
                for (int j = 0; j < 4; ++j) dma<AUX_X>(rx, l + 32768 + (w4 * 4 + j) * 1024, (j & 1) ? vb1 : vb0, sb + (unsigned)(k * 64 + j * 8) * 2048);
#pragma unroll
                for (int j = 0; j < 8; ++j) dma<AUX_C>(rc, l + (w4 * 8 + j) * 1024, (j & 1) ? va1 : va0, sa + (unsigned)j * 8 * 4096 + (unsigned)k * 128);
            }, DBGP(4));
        }
        STAMP(1);
        BAR();
        if (w4 == 0) {
            const u64 tag = mix(ep);
            if (lane == 0) st_dev(fl + FL_U + g * 128 + i16 * 8, tag);
            poll(fl + FL_U + g * 128, 16, tag, lane);
            if (i16 == 0 && lane == 0) st_dev(fl + FL_EB + g * 128, tag);
        }
        STAMP(2);
        BAR();
        {
            const rsrc_t ru = mk(ws + WS_U, 8u << 20), rh0 = mk(hr, 1024 * 1024 * 2), rh1 = mk(hi, 1024 * 1024 * 2);
            const unsigned vl = lane * 16;
            const unsigned vb0 = vo_rows(lane, 2048), vb1 = vb0 ^ 64;
            const unsigned su = (unsigned)(g * 16 + hh * 8 + w4 * 2) * 65536;
            const unsigned sb = (unsigned)(et * 128 + w4 * 32) * 2048;
            const int rot = ROT2;
            loader_loop<4, 8, 32>([&](int k0, int slot) {
                const int k = (k0 + rot) & 31;
                LAS char* l = lds + slot * ST2;
                const rsrc_t rh = k < 16 ? rh0 : rh1;
#pragma unroll
                for (int j = 0; j < 4; ++j) dma<AUX_H>(rh, l + 16384 + (w4 * 4 + j) * 1024, (j & 1) ? vb1 : vb0, sb + (unsigned)j * 8 * 2048 + (unsigned)(k & 15) * 128);
#pragma unroll
                for (int j = 0; j < 4; ++j) dma<AUX_I>(ru, l + (w4 * 4 + j) * 1024, vl, su + (unsigned)(j >> 1) * 65536 + (unsigned)(k * 2 + (j & 1)) * 1024);
            }, DBGP(10));
        }
        STAMP(3);
        BAR();
        stage_store(lds, out, (unsigned)(g * 256 + hh * 128) * 4096 + 2048 + et * 256, wid, lane);
    } else {
        const int wm = wid >> 1, wn = wid & 1, fr = lane & 15, fq = lane >> 4;
        {
            f32x4 acc[8][4];
            bf16x8 a0[8], b0[4];
            zero_acc(acc);
            const int ra0 = (wm * 128 + fr) * 128 + ((fq ^ (fr >> 1)) * 16), ra1 = ra0 ^ 64;
            int rb[4];
#pragma unroll
            for (int c = 0; c < 4; ++c) rb[c] = 32768 + wn * 8192 + tr_base(lane, c);
            auto rdA = [&](int m, int kh, int so) { return ld128(lds + so + (kh ? ra1 : ra0) + m * 2048); };
            auto rdB = [&](int n, int kh, int t, int so) { return ldtr(lds + so + rb[n] + kh * 4096 + t * 512); };
            unsigned sink = 0;
            const u32x4 tc = srd(part ? ci : cr, 2048 * 2048 * 2), tx = srd(bx, 2048 * 1024 * 2);
            const unsigned vtc = (unsigned)(g * 256 + dt * 32 + (lane >> 1)) * 4096 + (lane & 1) * 64;
            const unsigned vtx = (unsigned)(((g & 1) * 2 + part) * 16 + (lane >> 2)) * 2048 + dt * 256 + (lane & 3) * 64;
            auto pf = [&](int k) {
                if (TD && k < 32) {
                    if (wid == 0) touch<(AUX_C & 2) != 0>(sink, vtc, tc, (unsigned)k * 128);
                    if (wid == 1) touch<(AUX_X & 2) != 0>(sink, vtx, tx, (unsigned)k * 131072);
                }
            };
            if (TD) for (int k = 3; k < TD; ++k) pf(k);
            BAR();
            STAMPC(4); STAMP(12);
            first_frags(a0, b0, rdA, rdB);
            int slot = 0;
            compute_loop<8, 4, 4, 1, 32, RG1 ? 2 : 3, ST1>(acc, a0, b0, slot, rdA, rdB, [&](int k) { pf(k + TD); });
            STAMPC(5); STAMP(13);
            touch_done(sink);
            const rsrc_t ru = mk(ws + WS_U, 8u << 20);
            const unsigned sg = part ? 0x80008000u : 0u;
            const unsigned so = (unsigned)((g * 16 + wm * 8) * 64 + part * 32 + dt * 4 + wn * 2) * 1024;
#pragma unroll
            for (int m = 0; m < 8; ++m)
#pragma unroll
                for (int np = 0; np < 2; ++np) {
                    u32x4 v = pk8(acc[m][2 * np], acc[m][2 * np + 1]);
                    v.x ^= sg; v.y ^= sg; v.z ^= sg; v.w ^= sg;
                    st128<AUX_DEV>(v, ru, lane * 16 + so + (unsigned)(m * 64 + np) * 1024);
                }
            WAIT_V(0);
            STAMP(6);
        }
        unsigned sink = 0;
        const u32x4 th0 = srd(hr, 1024 * 1024 * 2), th1 = srd(hi, 1024 * 1024 * 2), tu = srd(ws + WS_U, 8u << 20);
        const unsigned vth = (unsigned)(et * 128 + ((g & 1) * 2 + hh) * 32 + (lane >> 1)) * 2048 + (lane & 1) * 64;
        const unsigned vtu = (unsigned)(g * 16 + hh * 8 + et) * 65536 + (lane & 31) * 64;
        auto pfh = [&](int k) { if (TD && k < 32 && wid == 0) touch<(AUX_H & 2) != 0>(sink, vth, k < 16 ? th0 : th1, (unsigned)(k & 15) * 128); };
        auto pfu = [&](int k) { if (TD && k < 32 && wid == 1) touch<0>(sink, vtu, tu, (unsigned)k * 2048); };
        if (TD) for (int k = 0; k < TD; ++k) pfh(k);
        BAR();
        BAR();
        if (TD) for (int k = 4; k < TD; ++k) pfu(k);
        {
            f32x4 acc[4][4];
            bf16x8 a0[4], b0[4];
            zero_acc(acc);
            const int ra = wm * 8192 + lane * 16;
            int rb[4];
#pragma unroll
            for (int c = 0; c < 4; ++c) rb[c] = 16384 + (wn * 64 + fr) * 128 + (((c * 2 + (fq >> 1)) ^ (fr >> 1)) * 16) + 8 * (fq & 1);
            auto rdA = [&](int m, int kh, int so) { return ld128(lds + so + ra + m * 2048 + kh * 1024); };
            auto rdB = [&](int n, int kh, int t, int so) { return ld64(lds + so + rb[kh * 2 + t] + n * 2048); };
            BAR();
            STAMPC(7); STAMP(14);
            first_frags(a0, b0, rdA, rdB);
            int slot = 0;
            compute_loop<4, 4, 4, 2, 32, RG2 ? 2 : 4, ST2>(acc, a0, b0, slot, rdA, rdB, [&](int k) { pfh(k + TD); pfu(k + TD); });
            STAMPC(8); STAMP(15);
            touch_done(sink);
            stage_write(lds, acc, wm, wn, lane);
            WAIT_L(0);
        }
        BAR();
        stage_store(lds, out, (unsigned)(g * 256 + hh * 128) * 4096 + 2048 + et * 256, wid, lane);
    }
    STAMP(9);
}

template <bool FIRST, int PAR>
__device__ __forceinline__ u32x4 attn_strip(LAS char* img, int s, bf16x8 qf, int lane) {
    constexpr float C = 0.17677669529663687f * 1.4426950408889634f;
    constexpr int JL = PAR, JD = PAR + 16;
    LAS char* p = img + (s & ~1) * 4096;
    const int r = lane & 15, g4 = (lane >> 4) * 4;
    f32x4 sc[18];
#pragma unroll
    for (int j = JL; j <= JD; ++j) sc[j] = MFMA(ld128(p + j * 4096), qf, ((f32x4){0.f, 0.f, 0.f, 0.f}));
    const float ninf = -__builtin_inff();
    f32x4 mg;
#pragma unroll
    for (int e = 0; e < 4; ++e) mg[e] = (g4 + e > r) ? (FIRST ? ninf : sc[JL][e]) : sc[JD][e];
    if (FIRST) {
#pragma unroll
        for (int j = JL + 1; j < JD; ++j) {
            const bool bad = (s & ~1) + j < 16;
#pragma unroll
            for (int e = 0; e < 4; ++e) sc[j][e] = bad ? ninf : sc[j][e];
        }
    }
    float mx = fmaxf(fmaxf(mg[0], mg[1]), fmaxf(mg[2], mg[3]));
#pragma unroll
    for (int j = JL + 1; j < JD; ++j) {
        mx = fmaxf(fmaxf(mx, sc[j][0]), sc[j][1]);
        mx = fmaxf(fmaxf(mx, sc[j][2]), sc[j][3]);
    }
    {
        auto a = __builtin_amdgcn_permlane16_swap(__float_as_uint(mx), __float_as_uint(mx), false, false);
        mx = fmaxf(__uint_as_float(a[0]), __uint_as_float(a[1]));
        auto b = __builtin_amdgcn_permlane32_swap(__float_as_uint(mx), __float_as_uint(mx), false, false);
        mx = fmaxf(__uint_as_float(b[0]), __uint_as_float(b[1]));
    }
    const float mc = -mx * C;
#pragma unroll
    for (int e = 0; e < 4; ++e) mg[e] = __builtin_amdgcn_exp2f(fmaf(mg[e], C, mc));
#pragma unroll
    for (int j = JL + 1; j < JD; ++j)
#pragma unroll
        for (int e = 0; e < 4; ++e) sc[j][e] = __builtin_amdgcn_exp2f(fmaf(sc[j][e], C, mc));
#pragma unroll
    for (int e = 0; e < 4; ++e) {
        const bool lo = g4 + e > r;
        sc[JL][e] = lo ? mg[e] : 0.f;
        sc[JD][e] = lo ? 0.f : mg[e];
    }
    sc[PAR ? 0 : 17] = (f32x4){0.f, 0.f, 0.f, 0.f};
    f32x4 o0 = {0.f, 0.f, 0.f, 0.f}, o1 = o0, sum = o0;
    const bf16x8 ones = {0x3F80, 0x3F80, 0x3F80, 0x3F80, 0x3F80, 0x3F80, 0x3F80, 0x3F80};
#pragma unroll
    for (int j2 = 0; j2 < 9; ++j2) {
        const bf16x8 pf = __builtin_bit_cast(bf16x8, pk8(sc[2 * j2], sc[2 * j2 + 1]));
        o0 = MFMA(ld128(p + (2 * j2) * 4096 + 2048), pf, o0);
        o1 = MFMA(ld128(p + (2 * j2 + 1) * 4096 + 2048), pf, o1);
        sum = MFMA(ones, pf, sum);
    }
    const float inv = 1.0f / sum[0];
    o0 *= inv;
    o1 *= inv;
    return pk8(o0, o1);
}

template <bool FIRST>
__device__ __forceinline__ void attention(LAS char* lds, int r, int hp, int wid, int lane, char* ws) {
    const int h = wid & 1, sp = wid >> 1;
    const int head = hp * 2 + h;
    const rsrc_t rq = mk(ws + WS_Q, 4u << 20), ro = mk(ws + WS_O, 4u << 20);
    LAS char* img = lds + 32768 + h * 1024 + lane * 16;
    bf16x8 qf[4];
#pragma unroll
    for (int i = 0; i < 4; ++i) {
        const int s = (i < 2 ? 8 : 0) + sp * 2 + (i & 1);
        qf[i] = __builtin_bit_cast(bf16x8, __builtin_amdgcn_raw_buffer_load_b128(rq, lane * 16, (unsigned)(head * 128 + r * 16 + s) * 1024, AUX_DEV));
    }
    WAIT_V(0);
    BAR();
#pragma unroll
    for (int i = 0; i < 2; ++i) {
        const int s0 = (i == 0 ? 8 : 0) + sp * 2;
        const u32x4 v0 = attn_strip<FIRST, 0>(img, s0, qf[2 * i], lane);
        st128<AUX_DEV>(v0, ro, lane * 16 + (unsigned)((r * 16 + s0) * 32 + head) * 1024);
        const u32x4 v1 = attn_strip<FIRST, 1>(img, s0 + 1, qf[2 * i + 1], lane);
        st128<AUX_DEV>(v1, ro, lane * 16 + (unsigned)((r * 16 + s0 + 1) * 32 + head) * 1024);
    }
    WAIT_V(0);
}

__device__ __forceinline__ void chain_a(LAS char* lds, int r, int hp, int wid, int lane, const char* ax, const char* wqkv, const char* wo,
                                        char* out, char* ws) {
    char* fl = ws + WS_FLAG;
    constexpr int ST1 = 40960, ST3 = 32768;
    u64 ep0 = 0, ep1 = 0;
    STAMP(0);
    if (wid >= 4) {
        const int w4 = wid - 4;
        if (w4 == 0) {
            ep0 = ld_dev(fl + FL_EA + r * 128);
            ep1 = ld_dev(fl + FL_EA + ((r + 1) & 7) * 128);
        }
        const rsrc_t rx = mk(ax, 2048 * 1024 * 2), rw = mk(wqkv, 1024 * 3072 * 2);
        const unsigned va0 = vo_rows(lane, 2048), va1 = va0 ^ 64;
        const unsigned vb0 = vo_tr(lane, (lane >> 3) * 6144), vb1 = vb0 ^ 64;
        const unsigned sa = (unsigned)(r * 256 + w4 * 32) * 2048;
        unsigned sb[6];
#pragma unroll
        for (int j = 0; j < 6; ++j) {
            const int i = w4 * 6 + j;
            sb[j] = (unsigned)((i & 7) * 8) * 6144 + (unsigned)((i >> 3) * 1024 + hp * 64) * 2;
        }
        const int rot = ROTQ;
        loader_loop<3, 10, 32>([&](int k, int slot) {
            LAS char* l = lds + slot * ST1;
            const unsigned kk = (k + rot) & 15, sub = (k >> 4) ^ 1;
#pragma unroll
            for (int j = 0; j < 6; ++j) dma<AUX_W>(rw, l + 16384 + (w4 * 6 + j) * 1024, (j & 1) ? vb1 : vb0, sb[j] + kk * 64 * 6144);
#pragma unroll
            for (int j = 0; j < 4; ++j) dma<AUX_AX>(rx, l + (w4 * 4 + j) * 1024, (j & 1) ? va1 : va0, sa + (sub * 128 + j * 8) * 2048 + kk * 128);
        }, DBGP(4));
        STAMP(1);
    } else {
        const int wm = wid >> 1, h = wid & 1, fr = lane & 15, fq = lane >> 4;
        f32x4 acc[4][6];
        bf16x8 a0[4], b0[6];
        zero_acc(acc);
        const int ra0 = (wm * 64 + fr) * 128 + ((fq ^ (fr >> 1)) * 16), ra1 = ra0 ^ 64;
        const int rb0 = 16384 + tr_base(lane, 2 * h), rb1 = 16384 + tr_base(lane, 2 * h + 1);
        auto rdA = [&](int m, int kh, int so) { return ld128(lds + so + (kh ? ra1 : ra0) + m * 2048); };
        auto rdB = [&](int n, int kh, int t, int so) { return ldtr(lds + so + ((n & 1) ? rb1 : rb0) + (n >> 1) * 8192 + kh * 4096 + t * 512); };
        const rsrc_t rq = mk(ws + WS_Q, 4u << 20), rkv = mk(ws + WS_KV, 8u << 20);
        unsigned sink = 0;
        const u32x4 tx = srd(ax, 2048 * 1024 * 2), tw = srd(wqkv, 1024 * 3072 * 2);
        const unsigned vtx = (unsigned)(r * 256 + (hp & 3) * 32 + (lane >> 1)) * 2048 + (lane & 1) * 64;
        const unsigned vtw = (unsigned)(r * 8 + ((lane >> 1) & 7)) * 6144 + (unsigned)(((lane >> 4) % 3) * 1024 + hp * 64) * 2 + (lane & 1) * 64;
        auto pf = [&](int k) {
            if (TDA && k < 32) {
                if (wid == 0) touch<(AUX_AX & 2) != 0>(sink, vtx, tx, (unsigned)(((k >> 4) ^ 1) * 128) * 2048 + (unsigned)(k & 15) * 128);
                if (wid == 1 && k < 16) touch<(AUX_W & 2) != 0>(sink, vtw, tw, (unsigned)k * 64 * 6144);
            }
        };
        if (TDA) for (int k = 3; k < TDA; ++k) pf(k);
        BAR();
        STAMPC(4); STAMP(12);
        first_frags(a0, b0, rdA, rdB);
        int slot = 0;
#pragma unroll 1
        for (int it = 0; it < 2; ++it) {
            compute_loop<4, 6, 4, 1, 16, RGQ ? 2 : 3, ST1>(acc, a0, b0, slot, rdA, rdB, [&](int k) { pf(it * 16 + k + TDA); });
            const int t0 = r * 16 + (it ^ 1) * 8 + wm * 4;
            const unsigned sq = (unsigned)((hp * 2 + h) * 128 + t0) * 1024;
            const unsigned sk = (unsigned)(hp * 128 + t0) * 4096 + (unsigned)h * 1024;
#pragma unroll
            for (int m = 0; m < 4; ++m) {
                st128<AUX_DEV>(pk8(acc[m][2], acc[m][3]), rkv, lane * 16 + sk + m * 4096);
                if (m & 1) {
                    st128<AUX_DEV>(pk8(acc[m - 1][4], acc[m][4]), rkv, lane * 16 + sk + (m - 1) * 4096 + 2048);
                    st128<AUX_DEV>(pk8(acc[m - 1][5], acc[m][5]), rkv, lane * 16 + sk + m * 4096 + 2048);
                }
                st128<AUX_DEV>(pk8(acc[m][0], acc[m][1]), rq, lane * 16 + sq + m * 1024);
            }
            zero_acc(acc);
        }
        STAMPC(5); STAMP(13);
        touch_done(sink);
        STAMP(6);
    }
    BAR();
    {
        const rsrc_t rkv = mk(ws + WS_KV, 8u << 20);
        const unsigned vl = lane * 16;
        const unsigned own = (unsigned)(hp * 128 + r * 16) * 4096;
        if (wid == 4) {
            if (lane == 0 && r < 7) st_dev(fl + FL_KV + r * 128 + hp * 8, mix(ep1));
        }
#pragma unroll
        for (int j = 0; j < 8; ++j) dma<AUX_DEV>(rkv, lds + 32768 + 65536 + (wid * 8 + j) * 1024, vl, own + (unsigned)(wid * 8 + j) * 1024);
        if (r > 0) {
            if (wid == 4) poll(fl + FL_KV + (r - 1) * 128 + hp * 8, 1, mix(ep0), lane);
            BAR();
#pragma unroll
            for (int j = 0; j < 8; ++j) dma<AUX_DEV>(rkv, lds + 32768 + (wid * 8 + j) * 1024, vl, own - 65536 + (unsigned)(wid * 8 + j) * 1024);
            STAMP(2);
            attention<false>(lds, r, hp, wid, lane, ws);
        } else {
#pragma unroll
            for (int j = 0; j < 8; ++j) *(LAS u32x4*)(lds + 32768 + (wid * 8 + j) * 1024 + vl) = (u32x4){0u, 0u, 0u, 0u};
            WAIT_L(0);
            STAMP(2);
            attention<true>(lds, r, hp, wid, lane, ws);
        }
    }
    STAMP(3);
    BAR();
    const int hh = hp & 1, ct = hp >> 1;
    const unsigned dst = (unsigned)(r * 256 + hh * 128) * 4096 + ct * 256;
    if (wid >= 4) {
        const int w4 = wid - 4;
        if (w4 == 0) {
            const u64 tag = mix(ep0);
            if (lane == 0) st_dev(fl + FL_O + r * 128 + hp * 8, tag);
            poll(fl + FL_O + r * 128, 16, tag, lane);
            if (hp == 0 && lane == 0) st_dev(fl + FL_EA + r * 128, tag);
        }
        STAMP(7);
        BAR();
        const rsrc_t ro = mk(ws + WS_O, 4u << 20), rw = mk(wo, 1024 * 1024 * 2);
        const unsigned vl = lane * 16;
        const unsigned vb0 = vo_tr(lane, (unsigned)(16 * (lane >> 5) + ((lane >> 3) & 3)) * 2048), vb1 = vb0 ^ 64;
        const unsigned so = (unsigned)(r * 16 + hh * 8 + w4 * 2) * 32768;
        const unsigned sb = (unsigned)((w4 & 1) * 32) * 2048 + (unsigned)(ct * 128 + (w4 >> 1) * 64) * 2;
        const int rot = ROTP;
        loader_loop<4, 8, 16>([&](int k0, int slot) {
            const int k = (k0 + rot) & 15;
            LAS char* l = lds + slot * ST3;
#pragma unroll
            for (int j = 0; j < 4; ++j) dma<AUX_WO>(rw, l + 16384 + (w4 * 4 + j) * 1024, (j & 1) ? vb1 : vb0, sb + (unsigned)(k * 64 + j * 4) * 2048);
#pragma unroll
            for (int j = 0; j < 4; ++j) dma<AUX_I>(ro, l + (w4 * 4 + j) * 1024, vl, so + (unsigned)(j >> 1) * 32768 + (unsigned)(k * 2 + (j & 1)) * 1024);
        }, DBGP(10));
        STAMP(8);
        BAR();
        stage_store(lds, out, dst, wid, lane);
    } else {
        const int wm = wid >> 1, wn = wid & 1;
        f32x4 acc[4][4];
        bf16x8 a0[4], b0[4];
        zero_acc(acc);
        const int ra = wm * 8192 + lane * 16;
        int rb[4];
#pragma unroll
        for (int c = 0; c < 4; ++c) rb[c] = 16384 + wn * 8192 + tr_base(lane, c);
        auto rdA = [&](int m, int kh, int so) { return ld128(lds + so + ra + m * 2048 + kh * 1024); };
        auto rdB = [&](int n, int kh, int t, int so) { return ldtr(lds + so + rb[n] + kh * 4096 + t * 512); };
        unsigned sink = 0;
        const u32x4 tw = srd(wo, 1024 * 1024 * 2), to = srd(ws + WS_O, 4u << 20);
        const unsigned vtw = (unsigned)((r * 2 + hh) * 4 + ((lane >> 2) & 3)) * 2048 + ct * 256 + (lane & 3) * 64;
        const unsigned vto = (unsigned)(r * 16 + hh * 8 + (ct & 1) * 4 + (lane >> 4)) * 32768 + (lane & 15) * 64;
        auto pfw = [&](int k) { if (TDA && k < 16 && wid == 0) touch<(AUX_WO & 2) != 0>(sink, vtw, tw, (unsigned)k * 64 * 2048); };
        auto pfo = [&](int k) { if (TDA && k < 16 && wid >= 1 && wid <= 2) touch<0>(sink, vto, to, (unsigned)k * 2048 + (wid - 1) * 1024); };
        if (TDA) for (int k = 0; k < TDA; ++k) pfw(k);
        BAR();
        BAR();
        if (TDA) for (int k = 4; k < TDA; ++k) pfo(k);
        STAMPC(10); STAMP(14);
        first_frags(a0, b0, rdA, rdB);
        int slot = 0;
        compute_loop<4, 4, 4, 2, 16, RGP ? 2 : 4, ST3>(acc, a0, b0, slot, rdA, rdB, [&](int k) { pfw(k + TDA); pfo(k + TDA); });
        STAMPC(11); STAMP(15);
        touch_done(sink);
        stage_write(lds, acc, wm, wn, lane);
        WAIT_L(0);
        BAR();
        stage_store(lds, out, dst, wid, lane);
    }
    STAMP(9);
}

__global__ __launch_bounds__(512, 2) void fused_kernel(const char* ax, const char* wqkv, const char* wo, const char* bx, const char* cr,
                                                       const char* ci, const char* hr, const char* hi, char* out, char* ws) {
    extern __shared__ __attribute__((aligned(16))) char smem[];
    LAS char* lds = (LAS char*)smem;
    const int wid = __builtin_amdgcn_readfirstlane(threadIdx.x >> 6), lane = threadIdx.x & 63;
    const int x = blockIdx.x & 7, idx = blockIdx.x >> 3;
    const int j = x >> 1;
    if (x & 1) chain_b(lds, 2 * j + (idx >> 4), idx & 15, wid, lane, bx, cr, ci, hr, hi, out, ws);
    else if (MAPA == 1) chain_a(lds, 2 * j + (idx >> 4), idx & 15, wid, lane, ax, wqkv, wo, out, ws);
    else chain_a(lds, idx >> 2, 4 * j + (idx & 3), wid, lane, ax, wqkv, wo, out, ws);
}

}

extern "C" void kernel_launch(void* const* d_in, const int* in_sizes, int n_in, void* d_out, int out_size, void* d_ws, size_t ws_size,
                              hipStream_t stream) {
    (void)in_sizes; (void)n_in; (void)out_size; (void)ws_size;
    (void)hipFuncSetAttribute((const void*)fused_kernel, hipFuncAttributeMaxDynamicSharedMemorySize, LDS_BYTES);
    fused_kernel<<<256, 512, LDS_BYTES, stream>>>((const char*)d_in[0], (const char*)d_in[1], (const char*)d_in[2], (const char*)d_in[3],
                                                  (const char*)d_in[4], (const char*)d_in[5], (const char*)d_in[6], (const char*)d_in[7],
                                                  (char*)d_out, (char*)d_ws);
}
```

```cpp
#include <hip/hip_runtime.h>
#include <stdint.h>

#define LAS __attribute__((address_space(3)))
typedef short bf16x8 __attribute__((ext_vector_type(8)));
typedef short s16x4 __attribute__((ext_vector_type(4)));
typedef float f32x2 __attribute__((ext_vector_type(2)));
typedef float f32x4 __attribute__((ext_vector_type(4)));
typedef __bf16 bfx2 __attribute__((ext_vector_type(2)));
typedef unsigned u32x4 __attribute__((ext_vector_type(4)));
typedef unsigned u32x2 __attribute__((ext_vector_type(2)));
typedef unsigned long long u64;
typedef __amdgpu_buffer_rsrc_t rsrc_t;

#define WAIT_V(n) asm volatile("s_waitcnt vmcnt(%0)" ::"n"(n) : "memory")
#define WAIT_L(n) asm volatile("s_waitcnt lgkmcnt(%0)" ::"n"(n) : "memory")
#define WAIT_L0() __builtin_amdgcn_s_waitcnt(0xC07F)
#define BAR() do { asm volatile("" ::: "memory"); __builtin_amdgcn_s_barrier(); asm volatile("" ::: "memory"); } while (0)
#define SCHED() __builtin_amdgcn_sched_barrier(0)
#define MFMA(a, b, c) __builtin_amdgcn_mfma_f32_16x16x32_bf16(a, b, c, 0, 0, 0)

#define STAMPS 0
#define MAPA 2
#define STAMP(i) do { } while (0)
#define STAMPC(i) do { } while (0)
#define DBGP(i) nullptr

namespace {

constexpr int AUX_DEV = 16;
constexpr unsigned WS_U = 0;
constexpr unsigned WS_KV = 8u << 20;
constexpr unsigned WS_Q = 16u << 20;
constexpr unsigned WS_O = 20u << 20;
constexpr unsigned WS_FLAG = 24u << 20;
constexpr unsigned WS_STAMP = 25u << 20;
constexpr unsigned WS_BYTES = 26u << 20;
constexpr unsigned FL_EB = 0, FL_EA = 1024, FL_U = 2048, FL_O = 3072, FL_KV = 4096;
constexpr int LDS_BYTES = 160 * 1024;
constexpr int SPIN_MAX = 100000;

__device__ __forceinline__ unsigned pk(float a, float b) {
    f32x2 v = {a, b};
    return __builtin_bit_cast(unsigned, __builtin_convertvector(v, bfx2));
}
__device__ __forceinline__ u32x4 pk8(const f32x4& a, const f32x4& b) {
    u32x4 r;
    r.x = pk(a[0], a[1]); r.y = pk(a[2], a[3]); r.z = pk(b[0], b[1]); r.w = pk(b[2], b[3]);
    return r;
}
__device__ __forceinline__ rsrc_t mk(const void* p, unsigned bytes) { return __builtin_amdgcn_make_buffer_rsrc((void*)p, 0, (int)bytes, 0x00020000); }
template <int AUX>
__device__ __forceinline__ void dma(rsrc_t r, LAS char* l, unsigned vo, unsigned so) {
    __builtin_amdgcn_raw_ptr_buffer_load_lds(r, (LAS void*)l, 16, (int)vo, (int)so, 0, AUX);
}
template <int AUX>
__device__ __forceinline__ void st128(u32x4 v, rsrc_t r, unsigned vo) { __builtin_amdgcn_raw_buffer_store_b128(v, r, (int)vo, 0, AUX); }
__device__ __forceinline__ bf16x8 ld128(LAS char* p) { return *(const LAS bf16x8*)p; }
__device__ __forceinline__ s16x4 ld64(LAS char* p) { return *(const LAS s16x4*)p; }
__device__ __forceinline__ s16x4 ldtr(LAS char* p) { return __builtin_amdgcn_ds_read_tr16_b64_v4i16((LAS s16x4*)p); }
__device__ __forceinline__ bf16x8 cat(s16x4 a, s16x4 b) { return __builtin_shufflevector(a, b, 0, 1, 2, 3, 4, 5, 6, 7); }

#define WSOFF 0
#define HP 3
#define PFH 2
#define AUX_C 0
#define AUX_AX 0
#define AUX_X 0
#define AUX_H 0
#define AUX_W 0
#define AUX_WO 0
#define AUX_I 16
#define LB1 8
#define LB2 8
#define LBQ 8
#define LBP 8
#define ROT1 (dt + 2 * (part * 2 + (g & 1)))
#define ROT2 (et + 2 * (hh * 2 + (g & 1)))
#define ROTQ (r + 2 * (hp & 3))
#define ROTP (r + 4 * (ct & 1))
#define PH1 0
#define PH2 0
#define PHQ 0
#define PHP 0
template <int NK, int LB>
__device__ __forceinline__ int kmap(int t, int rot, int ph) {
    const int u = (t + ph) & (NK - 1);
    return (u & ~(LB - 1)) | ((u + rot) & (LB - 1));
}
__device__ __forceinline__ u64 mix(u64 z) {
    z += 0x9E3779B97F4A7C15ull;
    z = (z ^ (z >> 30)) * 0xBF58476D1CE4E5B9ull;
    z = (z ^ (z >> 27)) * 0x94D049BB133111EBull;
    return z ^ (z >> 31);
}
__device__ __forceinline__ u64 ld_dev(const char* p) { return __hip_atomic_load((const u64*)p, __ATOMIC_RELAXED, __HIP_MEMORY_SCOPE_AGENT); }
__device__ __forceinline__ void st_dev(char* p, u64 v) { __hip_atomic_store((u64*)p, v, __ATOMIC_RELAXED, __HIP_MEMORY_SCOPE_AGENT); }
__device__ __forceinline__ void poll(const char* base, int n, u64 tag, int lane) {
    const char* p = base + (lane & (n - 1)) * 8;
    for (int it = 0; it < SPIN_MAX; ++it) {
        const u64 v = ld_dev(p);
        if (__all(v == tag)) break;
        __builtin_amdgcn_s_sleep(2);
    }
}

template <int S, int P, int NK, class ISSUE>
__device__ __forceinline__ void loader_steady(ISSUE issue, int k0, int slot, u64* dbg) {
    for (int k = k0; k < NK - S; ++k) {
        WAIT_V((S - 2) * P);
        BAR();
        issue(k + S, slot);
        slot = slot == S - 1 ? 0 : slot + 1;
    }
#pragma unroll
    for (int t = 0; t < S; ++t) {
        WAIT_V((S - 2 - t > 0 ? S - 2 - t : 0) * P);
        BAR();
    }
}
template <int S, int P, int NK, class ISSUE>
__device__ __forceinline__ void loader_loop(ISSUE issue, u64* dbg = nullptr) {
#pragma unroll
    for (int s = 0; s < S; ++s) issue(s, s);
    WAIT_V((S - 1) * P);
    BAR();
    loader_steady<S, P, NK>(issue, 0, 0, dbg);
}
template <int S, int PA, int PB, int NK, class ISSUE>
__device__ __forceinline__ void loader_tail(ISSUE issue, u64* dbg = nullptr) {
    constexpr int P = PA + PB;
    WAIT_V((S - 1) * PB);
    BAR();
#pragma unroll
    for (int k = 0; k < S - 2; ++k) {
        WAIT_V((S - 2 - k) * PB + k * P);
        BAR();
        issue(k + S, k);
    }
    loader_steady<S, P, NK>(issue, S - 2, S - 2, dbg);
}

template <int MT, int NT, int SWAP0, bool SYNC, class FA, class FB>
__device__ __forceinline__ void half_step(f32x4 (&acc)[MT][NT], const bf16x8 (&ac)[MT], const bf16x8 (&bc)[NT],
                                          bf16x8 (&an)[MT], bf16x8 (&bn)[NT], FA rdA, FB rdB) {
    s16x4 lo[NT];
#pragma unroll
    for (int m = 0; m < MT; ++m) {
#pragma unroll
        for (int n = 0; n < NT; ++n) {
            const int i = m * NT + n;
            acc[m][n] = (n >= SWAP0) ? MFMA(ac[m], bc[n], acc[m][n]) : MFMA(bc[n], ac[m], acc[m][n]);
            SCHED();
            if (i < 2 * NT) {
                if ((i & 1) == 0) lo[i >> 1] = rdB(i >> 1, 0);
                else bn[i >> 1] = cat(lo[i >> 1], rdB(i >> 1, 1));
                SCHED();
            } else if (i - 2 * NT < MT) {
                an[i - 2 * NT] = rdA(i - 2 * NT);
                SCHED();
            }
        }
    }
    WAIT_L0();
    if (SYNC) BAR();
    SCHED();
}

template <int MT, int NT, int SWAP0, int NK, int S, int S0, class FA, class FB>
__device__ __forceinline__ void compute_loop(f32x4 (&acc)[MT][NT], bf16x8 (&a0)[MT], bf16x8 (&b0)[NT], FA rdA, FB rdB) {
    bf16x8 a1[MT], b1[NT];
    auto step = [&](int s) {
        const int sn = s == S - 1 ? 0 : s + 1;
        half_step<MT, NT, SWAP0, true>(acc, a0, b0, a1, b1, [&](int m) { return rdA(s, 1, m); }, [&](int n, int t) { return rdB(s, 1, n, t); });
        half_step<MT, NT, SWAP0, false>(acc, a1, b1, a0, b0, [&](int m) { return rdA(sn, 0, m); }, [&](int n, int t) { return rdB(sn, 0, n, t); });
    };
#pragma unroll 1
    for (int it = 0; it < NK / S; ++it) {
#pragma unroll
        for (int j = 0; j < S; ++j) step((S0 + j) % S);
    }
#pragma unroll
    for (int j = 0; j < NK % S; ++j) step((S0 + j) % S);
}
template <int MT, int NT, class FA, class FB>
__device__ __forceinline__ void first_frags(bf16x8 (&a0)[MT], bf16x8 (&b0)[NT], FA rdA, FB rdB) {
#pragma unroll
    for (int n = 0; n < NT; ++n) b0[n] = cat(rdB(0, 0, n, 0), rdB(0, 0, n, 1));
#pragma unroll
    for (int m = 0; m < MT; ++m) a0[m] = rdA(0, 0, m);
    WAIT_L0();
    SCHED();
}
template <int MT, int NT>
__device__ __forceinline__ void zero_acc(f32x4 (&acc)[MT][NT]) {
#pragma unroll
    for (int m = 0; m < MT; ++m)
#pragma unroll
        for (int n = 0; n < NT; ++n) acc[m][n] = (f32x4){0.f, 0.f, 0.f, 0.f};
}
__device__ __forceinline__ LAS char* pin(LAS char* p) { asm volatile("" : "+v"(p)); return p; }

__device__ __forceinline__ unsigned vo_rows(int lane, unsigned ld) { return (unsigned)(lane >> 3) * ld + (unsigned)(((lane & 7) ^ (lane >> 4)) * 16); }
__device__ __forceinline__ unsigned vo_tr(int lane, unsigned rowoff) { return rowoff + (unsigned)(((lane & 7) ^ (((lane >> 4) & 1) << 1)) * 16); }
__device__ __forceinline__ int tr_base(int lane, int c) {
    const int g = lane >> 4, q = (lane & 15) >> 2, p = lane & 3;
    const int f = ((q >> 1) & 1) | ((g & 1) << 1);
    return 128 * (8 * g + q) + ((((c ^ f) << 1) | (p >> 1)) * 16) + 8 * (p & 1);
}

__device__ __forceinline__ void stage_write(LAS char* lds, const f32x4 (&acc)[4][4], int wm, int wn, int lane) {
    const int fr = lane & 15, g = lane >> 4;
#pragma unroll
    for (int m = 0; m < 4; ++m)
#pragma unroll
        for (int n = 0; n < 4; ++n) {
            const int row = wm * 64 + m * 16 + fr;
            const int ch = (wn * 8 + 2 * n + (g >> 1)) ^ ((fr >> 1) & 7);
            u32x2 w;
            w.x = pk(acc[m][n][0], acc[m][n][1]);
            w.y = pk(acc[m][n][2], acc[m][n][3]);
            *(LAS u32x2*)(lds + row * 256 + ch * 16 + 8 * (g & 1)) = w;
        }
}
#define OUTAUX 16
__device__ __forceinline__ void stage_store(LAS char* lds, char* out, unsigned dst, int wid, int lane) {
    const rsrc_t ro = mk(out, 2048 * 2048 * 2);
#pragma unroll
    for (int i = 0; i < 4; ++i) {
        const int row = wid * 16 + i * 4 + (lane >> 4);
        const int ch = (lane & 15) ^ ((row >> 1) & 7);
        const u32x4 v = *(const LAS u32x4*)(lds + row * 256 + ch * 16);
        st128<OUTAUX>(v, ro, dst + (unsigned)row * 4096 + (lane & 15) * 16);
    }
}

__device__ __forceinline__ void chain_b(LAS char* lds, int g, int i16, int wid, int lane, const char* bx, const char* cr, const char* ci,
                                        const char* hr, const char* hi, char* out, char* ws) {
    const int part = i16 >> 3, dt = i16 & 7;
    const int hh = i16 & 1, et = i16 >> 1;
    char* fl = ws + WS_FLAG;
    STAMP(0);
    if (wid >= 4) {
        const int w4 = wid - 4;
        u64 ep = 0;
        if (w4 == 0) ep = ld_dev(fl + FL_EB + g * 128);
        {
            const rsrc_t rc = mk(part ? ci : cr, 2048 * 2048 * 2), rx = mk(bx, 2048 * 1024 * 2);
            const unsigned va0 = vo_rows(lane, 4096), va1 = va0 ^ 64;
            const unsigned vb0 = vo_tr(lane, (lane >> 3) * 2048), vb1 = vb0 ^ 64;
            const unsigned sa = (unsigned)(g * 256 + w4 * 64) * 4096;
            const unsigned sb = (unsigned)((w4 & 1) * 32) * 2048 + (unsigned)(dt * 128 + (w4 >> 1) * 64) * 2;
            const int rot = ROT1, ph = PH1;
            loader_loop<3, 12, 32>([&](int k0, int slot) {
                const int k = kmap<32, LB1>(k0, rot, ph);
                LAS char* la = lds + slot * 32768 + w4 * 8192;
                LAS char* lb = lds + 98304 + slot * 16384 + w4 * 4096;
#pragma unroll
                for (int j = 0; j < 4; ++j) dma<AUX_X>(rx, lb + j * 1024, (j & 1) ? vb1 : vb0, sb + (unsigned)(k * 64 + j * 8) * 2048);
#pragma unroll
                for (int j = 0; j < 8; ++j) dma<AUX_C>(rc, la + j * 1024, (j & 1) ? va1 : va0, sa + (unsigned)j * 8 * 4096 + (unsigned)k * 128);
            }, DBGP(4));
        }
        STAMP(1);
        const rsrc_t ru = mk(ws + WS_U, 8u << 20), rh0 = mk(hr, 1024 * 1024 * 2), rh1 = mk(hi, 1024 * 1024 * 2);
        const unsigned vl = lane * 16;
        const unsigned vb0 = vo_rows(lane, 2048), vb1 = vb0 ^ 64;
        const unsigned su = (unsigned)(g * 16 + hh * 8 + w4 * 2) * 65536;
        const unsigned sb = (unsigned)(et * 128 + w4 * 32) * 2048;
        const int rot = ROT2, ph = PH2;
        auto issue_h = [&](int k0, int slot) {
            const int k = kmap<32, LB2>(k0, rot, ph);
            LAS char* l = lds + 65536 + slot * 16384 + w4 * 4096;
            const rsrc_t rh = k < 16 ? rh0 : rh1;
#pragma unroll
            for (int j = 0; j < 4; ++j) dma<AUX_H>(rh, l + j * 1024, (j & 1) ? vb1 : vb0, sb + (unsigned)j * 8 * 2048 + (unsigned)(k & 15) * 128);
        };
        auto issue_u = [&](int k0, int slot) {
            const int k = kmap<32, LB2>(k0, rot, ph);
            LAS char* l = lds + slot * 16384 + w4 * 4096;
#pragma unroll
            for (int j = 0; j < 4; ++j) dma<AUX_I>(ru, l + j * 1024, vl, su + (unsigned)(j >> 1) * 65536 + (unsigned)(k * 2 + (j & 1)) * 1024);
        };
        BAR();
        if (w4 == 0 && lane == 0) st_dev(fl + FL_U + g * 128 + i16 * 8, mix(ep));
#pragma unroll
        for (int s = 0; s < 4; ++s) issue_h(s, s);
        if (w4 == 0) {
            const u64 tag = mix(ep);
            poll(fl + FL_U + g * 128, 16, tag, lane);
            if (i16 == 0 && lane == 0) st_dev(fl + FL_EB + g * 128, tag);
        }
        STAMP(2);
        BAR();
#pragma unroll
        for (int s = 0; s < 4; ++s) issue_u(s, s);
        loader_tail<4, 4, 4, 32>([&](int k, int slot) { issue_h(k, slot); issue_u(k, slot); }, DBGP(10));
        STAMP(3);
        BAR();
        stage_store(lds, out, (unsigned)(g * 256 + hh * 128) * 4096 + 2048 + et * 256, wid, lane);
    } else {
        const int wm = wid >> 1, wn = wid & 1, fr = lane & 15, fq = lane >> 4;
        {
            f32x4 acc[8][4];
            bf16x8 a0[8], b0[4];
            zero_acc(acc);
            const int ra0 = (wm * 128 + fr) * 128 + ((fq ^ (fr >> 1)) * 16), ra1 = ra0 ^ 64;
            LAS char* pa[2][2] = {{pin(lds + ra0), pin(lds + ra1)}, {pin(lds + 65536 + ra0), pin(lds + 65536 + ra1)}};
            LAS char* pb[4];
#pragma unroll
            for (int c = 0; c < 4; ++c) pb[c] = pin(lds + 98304 + wn * 8192 + tr_base(lane, c));
            auto rdA = [&](int s, int kh, int m) { return ld128(pa[s >> 1][kh] + (s & 1) * 32768 + m * 2048); };
            auto rdB = [&](int s, int kh, int n, int t) { return ldtr(pb[n] + s * 16384 + kh * 4096 + t * 512); };
            BAR();
            STAMPC(4); STAMP(12);
            first_frags(a0, b0, rdA, rdB);
            const rsrc_t ru = mk(ws + WS_U, 8u << 20);
            const unsigned so = (unsigned)((g * 16 + wm * 8) * 64 + part * 32 + dt * 4 + wn * 2) * 1024;
            compute_loop<8, 4, 4, 32, 3, 0>(acc, a0, b0, rdA, rdB);
            STAMPC(5); STAMP(13);
            const unsigned sg = part ? 0x80008000u : 0u;
#pragma unroll
            for (int m = 0; m < 8; ++m)
#pragma unroll
                for (int np = 0; np < 2; ++np) {
                    u32x4 v = pk8(acc[m][2 * np], acc[m][2 * np + 1]);
                    v.x ^= sg; v.y ^= sg; v.z ^= sg; v.w ^= sg;
                    st128<AUX_DEV>(v, ru, lane * 16 + so + (unsigned)(m * 64 + np) * 1024);
                }
            WAIT_V(0);
            STAMP(6);
        }
        BAR();
        BAR();
        {
            f32x4 acc[4][4];
            bf16x8 a0[4], b0[4];
            zero_acc(acc);
            LAS char* pa = pin(lds + wm * 8192 + lane * 16);
            LAS char* pb[4];
#pragma unroll
            for (int c = 0; c < 4; ++c) pb[c] = pin(lds + 65536 + (wn * 64 + fr) * 128 + (((c * 2 + (fq >> 1)) ^ (fr >> 1)) * 16) + 8 * (fq & 1));
            auto rdA = [&](int s, int kh, int m) { return ld128(pa + s * 16384 + m * 2048 + kh * 1024); };
            auto rdB = [&](int s, int kh, int n, int t) { return ld64(pb[kh * 2 + t] + s * 16384 + n * 2048); };
            BAR();
            STAMPC(7); STAMP(14);
            first_frags(a0, b0, rdA, rdB);
            compute_loop<4, 4, 4, 32, 4, 0>(acc, a0, b0, rdA, rdB);
            STAMPC(8); STAMP(15);
            stage_write(lds, acc, wm, wn, lane);
            WAIT_L(0);
        }
        BAR();
        stage_store(lds, out, (unsigned)(g * 256 + hh * 128) * 4096 + 2048 + et * 256, wid, lane);
    }
    STAMP(9);
}

template <bool FIRST, int PAR>
__device__ __forceinline__ u32x4 attn_strip(LAS char* img, int s, bf16x8 qf, int lane) {
    constexpr float C = 0.17677669529663687f * 1.4426950408889634f;
    constexpr int JL = PAR, JD = PAR + 16;
    LAS char* p = img + (s & ~1) * 4096;
    const int r = lane & 15, g4 = (lane >> 4) * 4;
    f32x4 sc[18];
#pragma unroll
    for (int j = JL; j <= JD; ++j) sc[j] = MFMA(ld128(p + j * 4096), qf, ((f32x4){0.f, 0.f, 0.f, 0.f}));
    const float ninf = -__builtin_inff();
    f32x4 mg;
#pragma unroll
    for (int e = 0; e < 4; ++e) mg[e] = (g4 + e > r) ? (FIRST ? ninf : sc[JL][e]) : sc[JD][e];
    if (FIRST) {
#pragma unroll
        for (int j = JL + 1; j < JD; ++j) {
            const bool bad = (s & ~1) + j < 16;
#pragma unroll
            for (int e = 0; e < 4; ++e) sc[j][e] = bad ? ninf : sc[j][e];
        }
    }
    float mx = fmaxf(fmaxf(mg[0], mg[1]), fmaxf(mg[2], mg[3]));
#pragma unroll
    for (int j = JL + 1; j < JD; ++j) {
        mx = fmaxf(fmaxf(mx, sc[j][0]), sc[j][1]);
        mx = fmaxf(fmaxf(mx, sc[j][2]), sc[j][3]);
    }
    {
        auto a = __builtin_amdgcn_permlane16_swap(__float_as_uint(mx), __float_as_uint(mx), false, false);
        mx = fmaxf(__uint_as_float(a[0]), __uint_as_float(a[1]));
        auto b = __builtin_amdgcn_permlane32_swap(__float_as_uint(mx), __float_as_uint(mx), false, false);
        mx = fmaxf(__uint_as_float(b[0]), __uint_as_float(b[1]));
    }
    const float mc = -mx * C;
#pragma unroll
    for (int e = 0; e < 4; ++e) mg[e] = __builtin_amdgcn_exp2f(fmaf(mg[e], C, mc));
#pragma unroll
    for (int j = JL + 1; j < JD; ++j)
#pragma unroll
        for (int e = 0; e < 4; ++e) sc[j][e] = __builtin_amdgcn_exp2f(fmaf(sc[j][e], C, mc));
#pragma unroll
    for (int e = 0; e < 4; ++e) {
        const bool lo = g4 + e > r;
        sc[JL][e] = lo ? mg[e] : 0.f;
        sc[JD][e] = lo ? 0.f : mg[e];
    }
    sc[PAR ? 0 : 17] = (f32x4){0.f, 0.f, 0.f, 0.f};
    f32x4 o0 = {0.f, 0.f, 0.f, 0.f}, o1 = o0, sum = o0;
    const bf16x8 ones = {0x3F80, 0x3F80, 0x3F80, 0x3F80, 0x3F80, 0x3F80, 0x3F80, 0x3F80};
#pragma unroll
    for (int j2 = 0; j2 < 9; ++j2) {
        const bf16x8 pf = __builtin_bit_cast(bf16x8, pk8(sc[2 * j2], sc[2 * j2 + 1]));
        o0 = MFMA(ld128(p + (2 * j2) * 4096 + 2048), pf, o0);
        o1 = MFMA(ld128(p + (2 * j2 + 1) * 4096 + 2048), pf, o1);
        sum = MFMA(ones, pf, sum);
    }
    const float inv = 1.0f / sum[0];
    o0 *= inv;
    o1 *= inv;
    return pk8(o0, o1);
}

template <bool FIRST>
__device__ __forceinline__ void attention(LAS char* lds, int r, int hp, int wid, int lane, char* ws, u64 tag_in, unsigned pf0, unsigned pf1) {
    const int h = wid & 1, sp = wid >> 1;
    const int head = hp * 2 + h;
    const rsrc_t ro = mk(ws + WS_O, 4u << 20);
    LAS char* img = lds + 32768 + h * 1024 + lane * 16;
    LAS char* qb = lds + (sp * 4 + h) * 1024 + lane * 16;
    const unsigned so = lane * 16 + (unsigned)((r * 16 + sp * 2) * 32 + head) * 1024;
    const u32x4 v0 = attn_strip<FIRST, 0>(img, 8 + sp * 2, ld128(qb + 16384), lane);
    if (!FIRST && wid >= 4) {
        poll(ws + WS_FLAG + FL_KV + ((r - 1) * 16 + hp) * 64 + 32, 4, tag_in, lane);
        const rsrc_t rkv = mk(ws + WS_KV, 8u << 20);
        const unsigned src = (unsigned)(hp * 128 + r * 16 - 16) * 4096 + (unsigned)(wid - 4) * 8192;
#pragma unroll
        for (int j = 0; j < 8; ++j) dma<AUX_DEV>(rkv, lds + 32768 + (wid - 4) * 8192 + j * 1024, lane * 16, src + j * 1024);
    }
    st128<AUX_DEV>(v0, ro, so + 8 * 32768);
    const u32x4 v1 = attn_strip<FIRST, 1>(img, 8 + sp * 2 + 1, ld128(qb + 16384 + 2048), lane);
    st128<AUX_DEV>(v1, ro, so + 9 * 32768);
    WAIT_V(2);
    asm volatile("" ::"v"(pf0), "v"(pf1));
    BAR();
    const u32x4 v2 = attn_strip<FIRST, 0>(img, sp * 2, ld128(qb), lane);
    st128<AUX_DEV>(v2, ro, so);
    const u32x4 v3 = attn_strip<FIRST, 1>(img, sp * 2 + 1, ld128(qb + 2048), lane);
    st128<AUX_DEV>(v3, ro, so + 32768);
    WAIT_V(0);
}

__device__ __forceinline__ u64 uni64(u64 v) {
    return ((u64)(unsigned)__builtin_amdgcn_readfirstlane((unsigned)(v >> 32)) << 32) | (unsigned)__builtin_amdgcn_readfirstlane((unsigned)v);
}
__device__ __forceinline__ void chain_a(LAS char* lds, int r, int hp, int wid, int lane, const char* ax, const char* wqkv, const char* wo,
                                        const char* hr, const char* hi, char* out, char* ws) {
    char* fl = ws + WS_FLAG;
    STAMP(0);
    const u64 ep0 = ld_dev(fl + FL_EA + r * 128);
    u64 tag_in, tag_out = 0;
    unsigned pf0 = 0, pf1 = 0;
    auto prefetch_later = [&]() {
        const int a = r * 16 + hp;
        pf0 = __builtin_amdgcn_raw_buffer_load_b32(mk(a < 64 ? hr : hi, 1024 * 1024 * 2), lane * 128, (unsigned)((a & 63) * 32768 + wid * 8192), 0);
        if (wid < 2) {
            const int i = (r * 4 + (hp & 3)) * 128 + wid * 64 + lane;
            pf1 = __builtin_amdgcn_raw_buffer_load_b32(mk(wo, 1024 * 1024 * 2), (unsigned)(i >> 2) * 2048 + (unsigned)(hp >> 2) * 512 + (i & 3) * 128, 0, 0);
        }
    };
    if (wid >= 4) {
        const int w4 = wid - 4;
        const rsrc_t rx = mk(ax, 2048 * 1024 * 2), rw = mk(wqkv, 1024 * 3072 * 2);
        const unsigned va0 = vo_rows(lane, 2048), va1 = va0 ^ 64;
        const unsigned vb0 = vo_tr(lane, (lane >> 3) * 6144), vb1 = vb0 ^ 64;
        const unsigned sa = (unsigned)(r * 256 + w4 * 32) * 2048;
        unsigned sb[6];
#pragma unroll
        for (int j = 0; j < 6; ++j) {
            const int i = w4 * 6 + j;
            sb[j] = (unsigned)((i & 7) * 8) * 6144 + (unsigned)((i >> 3) * 1024 + hp * 64) * 2;
        }
        const int rot = ROTQ, ph = PHQ;
        loader_loop<3, 10, 32>([&](int k, int slot) {
            LAS char* la = lds + slot * 16384 + w4 * 4096;
            LAS char* lb = lds + 49152 + slot * 24576 + w4 * 6144;
            const unsigned kk = kmap<16, LBQ>(k, rot, ph), sub = (k >> 4) ^ 1;
#pragma unroll
            for (int j = 0; j < 6; ++j) dma<AUX_W>(rw, lb + j * 1024, (j & 1) ? vb1 : vb0, sb[j] + kk * 64 * 6144);
#pragma unroll
            for (int j = 0; j < 4; ++j) dma<AUX_AX>(rx, la + j * 1024, (j & 1) ? va1 : va0, sa + (sub * 128 + j * 8) * 2048 + kk * 128);
        }, DBGP(4));
        STAMP(1);
        tag_in = uni64(mix(ep0));
        if (r > 0) {
            poll(fl + FL_KV + ((r - 1) * 16 + hp) * 64, 4, tag_in, lane);
            const rsrc_t rkv = mk(ws + WS_KV, 8u << 20);
            const unsigned src = (unsigned)(hp * 128 + r * 16 - 8) * 4096 + (unsigned)w4 * 8192;
#pragma unroll
            for (int j = 0; j < 8; ++j) dma<AUX_DEV>(rkv, lds + 65536 + w4 * 8192 + j * 1024, lane * 16, src + j * 1024);
        } else {
#pragma unroll
            for (int j = 0; j < 16; ++j) *(LAS u32x4*)(lds + 32768 + (w4 * 16 + j) * 1024 + lane * 16) = (u32x4){0u, 0u, 0u, 0u};
        }
        WAIT_V(0);
        WAIT_L(0);
        BAR();
    } else {
        const u64 ep1 = ld_dev(fl + FL_EA + ((r + 1) & 7) * 128);
        tag_in = uni64(mix(ep0));
        tag_out = uni64(mix(ep1));
        const int wm = wid >> 1, h = wid & 1, fr = lane & 15, fq = lane >> 4;
        f32x4 acc[4][6];
        bf16x8 a0[4], b0[6];
        zero_acc(acc);
        const int ra0 = (wm * 64 + fr) * 128 + ((fq ^ (fr >> 1)) * 16);
        LAS char* pa[2] = {pin(lds + ra0), pin(lds + (ra0 ^ 64))};
        const int rb0 = 49152 + tr_base(lane, 2 * h), rb1 = 49152 + tr_base(lane, 2 * h + 1);
        LAS char* pb[2][2] = {{pin(lds + rb0), pin(lds + rb1)}, {pin(lds + 49152 + rb0), pin(lds + 49152 + rb1)}};
        auto rdA = [&](int s, int kh, int m) { return ld128(pa[kh] + s * 16384 + m * 2048); };
        auto rdB = [&](int s, int kh, int n, int t) { return ldtr(pb[s >> 1][n & 1] + (s & 1) * 24576 + (n >> 1) * 8192 + kh * 4096 + t * 512); };
        const rsrc_t rkv = mk(ws + WS_KV, 8u << 20);
        LAS char* img = lds + 32768 + 65536 + wm * 16384 + h * 1024 + lane * 16;
        const unsigned gkv = lane * 16 + (unsigned)(hp * 128 + r * 16 + wm * 4) * 4096 + (unsigned)h * 1024;
        char* fkv = fl + FL_KV + (r * 16 + hp) * 64 + wid * 8;
        auto put_kv = [&](int half) {
#pragma unroll
            for (int m = 0; m < 4; ++m) {
                const u32x4 kf = pk8(acc[m][2], acc[m][3]);
                const u32x4 vf = (m & 1) ? pk8(acc[m - 1][5], acc[m][5]) : pk8(acc[m][4], acc[m + 1 < 4 ? m + 1 : 3][4]);
                const unsigned o = (unsigned)(half * 8 + m) * 4096;
                if (r < 7) {
                    st128<AUX_DEV>(kf, rkv, gkv + o);
                    st128<AUX_DEV>(vf, rkv, gkv + o + 2048);
                }
                *(LAS u32x4*)(img + o) = kf;
                *(LAS u32x4*)(img + o + 2048) = vf;
            }
        };
        BAR();
        STAMPC(4); STAMP(12);
        first_frags(a0, b0, rdA, rdB);
        compute_loop<4, 6, 4, 16, 3, 0>(acc, a0, b0, rdA, rdB);
        if (PFH == 3) prefetch_later();
        put_kv(1);
        u32x4 q1[4];
#pragma unroll
        for (int m = 0; m < 4; ++m) q1[m] = pk8(acc[m][0], acc[m][1]);
        zero_acc(acc);
        compute_loop<4, 6, 4, 6, 3, 1>(acc, a0, b0, rdA, rdB);
        WAIT_V(0);
        if (r < 7 && lane == 0) st_dev(fkv, tag_out);
        if (PFH == 2) prefetch_later();
        compute_loop<4, 6, 4, 10, 3, 1>(acc, a0, b0, rdA, rdB);
        STAMPC(5); STAMP(13);
        put_kv(0);
        LAS char* qb = lds + (wm * 8 + h) * 1024 + lane * 16;
#pragma unroll
        for (int m = 0; m < 4; ++m) {
            *(LAS u32x4*)(qb + m * 2048) = pk8(acc[m][0], acc[m][1]);
            *(LAS u32x4*)(qb + 16384 + m * 2048) = q1[m];
        }
        WAIT_L(0);
        STAMP(6);
        BAR();
        WAIT_V(0);
        if (r < 7 && lane == 0) st_dev(fkv + 32, tag_out);
    }
    STAMP(2);
    if (PFH == 1 && wid < 4) prefetch_later();
    if (r > 0) attention<false>(lds, r, hp, wid, lane, ws, tag_in, pf0, pf1);
    else attention<true>(lds, r, hp, wid, lane, ws, tag_in, pf0, pf1);
    STAMP(3);
    BAR();
    const int hh = hp & 1, ct = hp >> 1;
    const unsigned dst = (unsigned)(r * 256 + hh * 128) * 4096 + ct * 256;
    if (wid >= 4) {
        const int w4 = wid - 4;
        const rsrc_t ro = mk(ws + WS_O, 4u << 20), rw = mk(wo, 1024 * 1024 * 2);
        const unsigned vl = lane * 16;
        const unsigned vb0 = vo_tr(lane, (unsigned)(16 * (lane >> 5) + ((lane >> 3) & 3)) * 2048), vb1 = vb0 ^ 64;
        const unsigned so = (unsigned)(r * 16 + hh * 8 + w4 * 2) * 32768;
        const unsigned sb = (unsigned)((w4 & 1) * 32) * 2048 + (unsigned)(ct * 128 + (w4 >> 1) * 64) * 2;
        const int rot = ROTP, ph = PHP;
        auto issue_w = [&](int k0, int slot) {
            const int k = kmap<16, LBP>(k0, rot, ph);
            LAS char* l = lds + 65536 + slot * 16384 + w4 * 4096;
#pragma unroll
            for (int j = 0; j < 4; ++j) dma<AUX_WO>(rw, l + j * 1024, (j & 1) ? vb1 : vb0, sb + (unsigned)(k * 64 + j * 4) * 2048);
        };
        auto issue_o = [&](int k0, int slot) {
            const int k = kmap<16, LBP>(k0, rot, ph);
            LAS char* l = lds + slot * 16384 + w4 * 4096;
#pragma unroll
            for (int j = 0; j < 4; ++j) dma<AUX_I>(ro, l + j * 1024, vl, so + (unsigned)(j >> 1) * 32768 + (unsigned)(k * 2 + (j & 1)) * 1024);
        };
        if (w4 == 0 && lane == 0) st_dev(fl + FL_O + r * 128 + hp * 8, tag_in);
#pragma unroll
        for (int s = 0; s < 4; ++s) issue_w(s, s);
        if (w4 == 0) {
            poll(fl + FL_O + r * 128, 16, tag_in, lane);
            if (hp == 0 && lane == 0) st_dev(fl + FL_EA + r * 128, tag_in);
        }
        STAMP(7);
        BAR();
#pragma unroll
        for (int s = 0; s < 4; ++s) issue_o(s, s);
        loader_tail<4, 4, 4, 16>([&](int k, int slot) { issue_w(k, slot); issue_o(k, slot); }, DBGP(10));
        STAMP(8);
        BAR();
        stage_store(lds, out, dst, wid, lane);
    } else {
        const int wm = wid >> 1, wn = wid & 1;
        f32x4 acc[4][4];
        bf16x8 a0[4], b0[4];
        zero_acc(acc);
        LAS char* pa = pin(lds + wm * 8192 + lane * 16);
        LAS char* pb[4];
#pragma unroll
        for (int c = 0; c < 4; ++c) pb[c] = pin(lds + 65536 + wn * 8192 + tr_base(lane, c));
        auto rdA = [&](int s, int kh, int m) { return ld128(pa + s * 16384 + m * 2048 + kh * 1024); };
        auto rdB = [&](int s, int kh, int n, int t) { return ldtr(pb[n] + s * 16384 + kh * 4096 + t * 512); };
        BAR();
        BAR();
        STAMPC(10); STAMP(14);
        first_frags(a0, b0, rdA, rdB);
        compute_loop<4, 4, 4, 16, 4, 0>(acc, a0, b0, rdA, rdB);
        STAMPC(11); STAMP(15);
        stage_write(lds, acc, wm, wn, lane);
        WAIT_L(0);
        BAR();
        stage_store(lds, out, dst, wid, lane);
    }
    STAMP(9);
}

__global__ __launch_bounds__(512, 2) void fused_kernel(const char* ax, const char* wqkv, const char* wo, const char* bx, const char* cr,
                                                       const char* ci, const char* hr, const char* hi, char* out, char* ws) {
    extern __shared__ __attribute__((aligned(16))) char smem[];
    LAS char* lds = (LAS char*)smem;
    const int wid = __builtin_amdgcn_readfirstlane(threadIdx.x >> 6), lane = threadIdx.x & 63;
    const int x = blockIdx.x & 7, idx = blockIdx.x >> 3;
    const int j = x >> 1;
    if (x & 1) chain_b(lds, 2 * j + (idx >> 4), idx & 15, wid, lane, bx, cr, ci, hr, hi, out, ws);
    else chain_a(lds, idx >> 2, 4 * j + (idx & 3), wid, lane, ax, wqkv, wo, hr, hi, out, ws);
}

}

extern "C" void kernel_launch(void* const* d_in, const int* in_sizes, int n_in, void* d_out, int out_size, void* d_ws, size_t ws_size,
                              hipStream_t stream) {
    (void)in_sizes; (void)n_in; (void)out_size;
    (void)hipFuncSetAttribute((const void*)fused_kernel, hipFuncAttributeMaxDynamicSharedMemorySize, LDS_BYTES);
    fused_kernel<<<256, 512, LDS_BYTES, stream>>>((const char*)d_in[0], (const char*)d_in[1], (const char*)d_in[2], (const char*)d_in[3],
                                                  (const char*)d_in[4], (const char*)d_in[5], (const char*)d_in[6], (const char*)d_in[7],
                                                  (char*)d_out, (char*)d_ws + WSOFF);
}
```

```cpp
#include <hip/hip_runtime.h>
#include <stdint.h>

#define LAS __attribute__((address_space(3)))
typedef short bf16x8 __attribute__((ext_vector_type(8)));
typedef short s16x4 __attribute__((ext_vector_type(4)));
typedef float f32x2 __attribute__((ext_vector_type(2)));
typedef float f32x4 __attribute__((ext_vector_type(4)));
typedef __bf16 bfx2 __attribute__((ext_vector_type(2)));
typedef unsigned u32x4 __attribute__((ext_vector_type(4)));
typedef unsigned u32x2 __attribute__((ext_vector_type(2)));
typedef unsigned long long u64;
typedef __amdgpu_buffer_rsrc_t rsrc_t;

#define WAIT_V(n) asm volatile("s_waitcnt vmcnt(%0)" ::"n"(n) : "memory")
#define WAIT_L(n) asm volatile("s_waitcnt lgkmcnt(%0)" ::"n"(n) : "memory")
#define WAIT_L0() __builtin_amdgcn_s_waitcnt(0xC07F)
#define BAR() do { asm volatile("" ::: "memory"); __builtin_amdgcn_s_barrier(); asm volatile("" ::: "memory"); } while (0)
#define SCHED() __builtin_amdgcn_sched_barrier(0)
#define MFMA(a, b, c) __builtin_amdgcn_mfma_f32_16x16x32_bf16(a, b, c, 0, 0, 0)

namespace {

constexpr int AUX_DEV = 16;
constexpr unsigned WS_U = 0;
constexpr unsigned WS_KV = 8u << 20;
constexpr unsigned WS_O = 20u << 20;
constexpr unsigned WS_FLAG = 24u << 20;
constexpr unsigned FL_EB = 0, FL_EA = 1024, FL_KV = 4096;
constexpr unsigned FL_U = 16384, FL_O = 32768;
constexpr int LDS_BYTES = 160 * 1024;
constexpr int SPIN_MAX = 100000;

__device__ __forceinline__ unsigned pk(float a, float b) {
    f32x2 v = {a, b};
    return __builtin_bit_cast(unsigned, __builtin_convertvector(v, bfx2));
}
__device__ __forceinline__ u32x4 pk8(const f32x4& a, const f32x4& b) {
    u32x4 r;
    r.x = pk(a[0], a[1]); r.y = pk(a[2], a[3]); r.z = pk(b[0], b[1]); r.w = pk(b[2], b[3]);
    return r;
}
__device__ __forceinline__ rsrc_t mk(const void* p, unsigned bytes) { return __builtin_amdgcn_make_buffer_rsrc((void*)p, 0, (int)bytes, 0x00020000); }
template <int AUX>
__device__ __forceinline__ void dma(rsrc_t r, LAS char* l, unsigned vo, unsigned so) {
    __builtin_amdgcn_raw_ptr_buffer_load_lds(r, (LAS void*)l, 16, (int)vo, (int)so, 0, AUX);
}
__device__ __forceinline__ void touch_far(const void* base, unsigned bytes, unsigned ldsoff, unsigned vo) {
    const u64 a = (u64)base;
    const u32x4 r = {(unsigned)a, (unsigned)(a >> 32) & 0xFFFFu, bytes, 0x00020000u};
    asm volatile("s_mov_b32 m0, %0\n\ts_nop 0\n\tbuffer_load_dword %1, %2, 0 offen lds" ::"s"(ldsoff), "v"(vo), "s"(r) : "m0");
}
struct CodeSpan { u64 base; unsigned bytes; };
__device__ __forceinline__ CodeSpan code_span() {
    u64 pc;
    unsigned n, d;
    asm volatile("s_getpc_b64 %0\n.Lfk_here%=:\n\ts_mov_b32 %1, .Lfk_end_a-.Lfk_begin\n\ts_mov_b32 %2, .Lfk_end_b-.Lfk_begin\n\t"
                 "s_max_i32 %1, %1, %2\n\ts_max_i32 %1, %1, 0\n\ts_mov_b32 %2, .Lfk_here%=-.Lfk_begin"
                 : "=s"(pc), "=&s"(n), "=&s"(d)::"scc");
    return CodeSpan{pc - (u64)(long long)(int)d, n};
}
__device__ __forceinline__ unsigned code_line(int idx, int lane) { return (unsigned)((lane >> 4) * 512 + idx * 16 + (lane & 15)) * 128; }
__device__ __forceinline__ void touch_code(unsigned ldsoff, int idx, int lane) {
    const CodeSpan c = code_span();
    touch_far((const void*)c.base, c.bytes, ldsoff, code_line(idx, lane));
}
template <int AUX>
__device__ __forceinline__ void st128(u32x4 v, rsrc_t r, unsigned vo) { __builtin_amdgcn_raw_buffer_store_b128(v, r, (int)vo, 0, AUX); }
__device__ __forceinline__ bf16x8 ld128(LAS char* p) { return *(const LAS bf16x8*)p; }
__device__ __forceinline__ s16x4 ld64(LAS char* p) { return *(const LAS s16x4*)p; }
__device__ __forceinline__ s16x4 ldtr(LAS char* p) { return __builtin_amdgcn_ds_read_tr16_b64_v4i16((LAS s16x4*)p); }
__device__ __forceinline__ bf16x8 cat(s16x4 a, s16x4 b) { return __builtin_shufflevector(a, b, 0, 1, 2, 3, 4, 5, 6, 7); }

constexpr int PRO = 2, PRO2 = 2;
constexpr int AUX_IN = 0;
constexpr int AUX_I = 16;
template <int NK>
__device__ __forceinline__ int kmap(int t, int rot) {
    const int u = t & (NK - 1);
    return (u & ~7) | ((u + rot) & 7);
}
constexpr int FRONT = 4;
constexpr int XB = 0xAA, XJ = 0xFA50;
template <int NK, int D>
__device__ __forceinline__ int front_order(int t, int w) {
    w &= 7;
    const int s0 = w >= D ? w : w + 8;
    if (((t + D) & 7) == w && t + D < NK) return t + D;
    const int lim = t + D < NK ? t + D : NK;
    const int cnt = lim > s0 ? (lim - s0 + 7) >> 3 : 0;
    const int i = t - cnt, j = i - s0;
    return j < 0 ? i : s0 + 1 + j + ((j * 37) >> 8);
}
__device__ __forceinline__ u64 mix(u64 z) {
    z += 0x9E3779B97F4A7C15ull;
    z = (z ^ (z >> 30)) * 0xBF58476D1CE4E5B9ull;
    z = (z ^ (z >> 27)) * 0x94D049BB133111EBull;
    return z ^ (z >> 31);
}
__device__ __forceinline__ u64 ld_dev(const char* p) { return __hip_atomic_load((const u64*)p, __ATOMIC_RELAXED, __HIP_MEMORY_SCOPE_AGENT); }
__device__ __forceinline__ u64 ld_dev_late(const char* p, int after = 0) {
    int z;
    asm volatile("v_mov_b32 %0, 0" : "=v"(z) : "v"(after));
    return ld_dev(p + z);
}
__device__ __forceinline__ void st_dev(char* p, u64 v) { __hip_atomic_store((u64*)p, v, __ATOMIC_RELAXED, __HIP_MEMORY_SCOPE_AGENT); }
__device__ __forceinline__ void poll(const char* base, int n, u64 tag, int lane) {
    const char* p = base + (lane & (n - 1)) * 8;
    for (int it = 0; it < SPIN_MAX; ++it) {
        u64 v = tag;
        if (lane < n) v = ld_dev(p);
        if (__all(v == tag)) break;
        __builtin_amdgcn_s_sleep(10);
    }
}
__device__ __forceinline__ void announce(char* fl, unsigned priv, int grp, int p, u64 tag, int lane) {
    if (lane < 16) st_dev(fl + priv + (grp * 16 + lane) * 128 + p * 8, tag);
}
__device__ __forceinline__ const char* wait_line(char* fl, unsigned priv, int grp, int p) {
    return fl + priv + (grp * 16 + p) * 128;
}
__device__ __forceinline__ u64 uni64(u64 v) {
    return ((u64)(unsigned)__builtin_amdgcn_readfirstlane((unsigned)(v >> 32)) << 32) | (unsigned)__builtin_amdgcn_readfirstlane((unsigned)v);
}

struct NoTail { __device__ __forceinline__ void operator()() const {} };
template <int S, int P, int NK, int NX = 0, class ISSUE, class TAIL = NoTail>
__device__ __forceinline__ void loader_steady(ISSUE issue, int k0, int slot, TAIL tail = TAIL()) {
    for (int k = k0; k < NK - S; ++k) {
        WAIT_V((S - 2) * P);
        BAR();
        issue(k + S, slot);
        slot = slot == S - 1 ? 0 : slot + 1;
    }
#pragma unroll
    for (int t = 0; t < S; ++t) {
        WAIT_V((S - 2 - t > 0 ? S - 2 - t : 0) * P + (t > 0 ? NX : 0));
        BAR();
        if (t == 0) tail();
    }
}
template <int S, int P, int NK, int NX = 0, class ISSUE, class TAIL = NoTail>
__device__ __forceinline__ void loader_loop(ISSUE issue, TAIL tail = TAIL()) {
    constexpr int S1 = PRO;
#pragma unroll
    for (int s = 0; s < S1; ++s) issue(s, s);
    WAIT_V((S1 - 1) * P);
    BAR();
#pragma unroll
    for (int s = S1; s < S; ++s) issue(s, s);
    loader_steady<S, P, NK, NX>(issue, 0, 0, tail);
}
template <int S, int PA, int PB, int NK, class ISSUE, class ISSUE2>
__device__ __forceinline__ void loader_tail(ISSUE issue, ISSUE2 issue2) {
    constexpr int P = PA + PB;
    constexpr int S1 = PRO2;
#pragma unroll
    for (int s = 0; s < S1; ++s) issue2(s, s);
    WAIT_V((S1 - 1) * PB);
    BAR();
#pragma unroll
    for (int s = S1; s < S; ++s) issue2(s, s);
#pragma unroll
    for (int k = 0; k < S - 2; ++k) {
        WAIT_V((S - 2 - k) * PB + k * P);
        BAR();
        issue(k + S, k);
    }
    loader_steady<S, P, NK>(issue, S - 2, S - 2);
}

template <int MT, int NT, int SWAP0, bool SYNC, bool READ = true, class FA, class FB>
__device__ __forceinline__ void half_step(f32x4 (&acc)[MT][NT], const bf16x8 (&ac)[MT], const bf16x8 (&bc)[NT],
                                          bf16x8 (&an)[MT], bf16x8 (&bn)[NT], FA rdA, FB rdB) {
    s16x4 lo[NT];
#pragma unroll
    for (int m = 0; m < MT; ++m) {
#pragma unroll
        for (int n = 0; n < NT; ++n) {
            const int i = m * NT + n;
            acc[m][n] = (n >= SWAP0) ? MFMA(ac[m], bc[n], acc[m][n]) : MFMA(bc[n], ac[m], acc[m][n]);
            SCHED();
            if (!READ) continue;
            if (i < 2 * NT) {
                if ((i & 1) == 0) lo[i >> 1] = rdB(i >> 1, 0);
                else bn[i >> 1] = cat(lo[i >> 1], rdB(i >> 1, 1));
                SCHED();
            } else if (i - 2 * NT < MT) {
                an[i - 2 * NT] = rdA(i - 2 * NT);
                SCHED();
            }
        }
    }
    WAIT_L0();
    if (SYNC) BAR();
    SCHED();
}

struct NoHook { __device__ __forceinline__ void operator()(int) const {} };
template <int MT, int NT, int SWAP0, int NK, int S, int S0, class FA, class FB, class HOOK = NoHook>
__device__ __forceinline__ void compute_loop(f32x4 (&acc)[MT][NT], bf16x8 (&a0)[MT], bf16x8 (&b0)[NT], FA rdA, FB rdB, HOOK hook = HOOK()) {
    bf16x8 a1[MT], b1[NT];
    auto step = [&](int s) {
        const int sn = s == S - 1 ? 0 : s + 1;
        half_step<MT, NT, SWAP0, true>(acc, a0, b0, a1, b1, [&](int m) { return rdA(s, 1, m); }, [&](int n, int t) { return rdB(s, 1, n, t); });
        half_step<MT, NT, SWAP0, false>(acc, a1, b1, a0, b0, [&](int m) { return rdA(sn, 0, m); }, [&](int n, int t) { return rdB(sn, 0, n, t); });
    };
#pragma unroll 1
    for (int it = 0; it < NK / S; ++it) {
        hook(it);
#pragma unroll
        for (int j = 0; j < S; ++j) step((S0 + j) % S);
    }
#pragma unroll
    for (int j = 0; j < NK % S; ++j) step((S0 + j) % S);
}
template <int S0 = 0, int MT, int NT, class FA, class FB>
__device__ __forceinline__ void first_frags(bf16x8 (&a0)[MT], bf16x8 (&b0)[NT], FA rdA, FB rdB) {
#pragma unroll
    for (int n = 0; n < NT; ++n) b0[n] = cat(rdB(S0, 0, n, 0), rdB(S0, 0, n, 1));
#pragma unroll
    for (int m = 0; m < MT; ++m) a0[m] = rdA(S0, 0, m);
    WAIT_L0();
    SCHED();
}
template <int MT, int NT, int SWAP0, int NS, class FA, class FB>
__device__ __forceinline__ void resident_steps(f32x4 (&acc)[MT][NT], FA rdA, FB rdB) {
    bf16x8 a0[MT], b0[NT], a1[MT], b1[NT];
    first_frags<0>(a0, b0, rdA, rdB);
#pragma unroll
    for (int s = 0; s < NS; ++s) {
        half_step<MT, NT, SWAP0, false>(acc, a0, b0, a1, b1, [&](int m) { return rdA(s, 1, m); }, [&](int n, int t) { return rdB(s, 1, n, t); });
        if (s + 1 < NS)
            half_step<MT, NT, SWAP0, false>(acc, a1, b1, a0, b0, [&](int m) { return rdA(s + 1, 0, m); }, [&](int n, int t) { return rdB(s + 1, 0, n, t); });
        else
            half_step<MT, NT, SWAP0, false, false>(acc, a1, b1, a0, b0, [&](int m) { return rdA(s, 0, m); }, [&](int n, int t) { return rdB(s, 0, n, t); });
    }
}
template <int MT, int NT>
__device__ __forceinline__ void zero_acc(f32x4 (&acc)[MT][NT]) {
#pragma unroll
    for (int m = 0; m < MT; ++m)
#pragma unroll
        for (int n = 0; n < NT; ++n) acc[m][n] = (f32x4){0.f, 0.f, 0.f, 0.f};
}
__device__ __forceinline__ LAS char* pin(LAS char* p) { asm volatile("" : "+v"(p)); return p; }

__device__ __forceinline__ unsigned vo_rows(int lane, unsigned ld) { return (unsigned)(lane >> 3) * ld + (unsigned)(((lane & 7) ^ (lane >> 4)) * 16); }
__device__ __forceinline__ unsigned vo_tr(int lane, unsigned rowoff) { return rowoff + (unsigned)(((lane & 7) ^ (((lane >> 4) & 1) << 1)) * 16); }
__device__ __forceinline__ int tr_base(int lane, int c) {
    const int g = lane >> 4, q = (lane & 15) >> 2, p = lane & 3;
    const int f = ((q >> 1) & 1) | ((g & 1) << 1);
    return 128 * (8 * g + q) + ((((c ^ f) << 1) | (p >> 1)) * 16) + 8 * (p & 1);
}

__device__ __forceinline__ void stage_write(LAS char* lds, const f32x4 (&acc)[4][4], int wm, int wn, int lane) {
    const int fr = lane & 15, g = lane >> 4;
#pragma unroll
    for (int m = 0; m < 4; ++m)
#pragma unroll
        for (int n = 0; n < 4; ++n) {
            const int row = wm * 64 + m * 16 + fr;
            const int ch = (wn * 8 + 2 * n + (g >> 1)) ^ ((fr >> 1) & 7);
            u32x2 w;
            w.x = pk(acc[m][n][0], acc[m][n][1]);
            w.y = pk(acc[m][n][2], acc[m][n][3]);
            *(LAS u32x2*)(lds + row * 256 + ch * 16 + 8 * (g & 1)) = w;
        }
}
__device__ __forceinline__ void stage_store(LAS char* lds, char* out, unsigned dst, int wid, int lane) {
    const rsrc_t ro = mk(out, 2048 * 2048 * 2);
#pragma unroll
    for (int i = 0; i < 4; ++i) {
        const int row = wid * 16 + i * 4 + (lane >> 4);
        const int ch = (lane & 15) ^ ((row >> 1) & 7);
        const u32x4 v = *(const LAS u32x4*)(lds + row * 256 + ch * 16);
        st128<AUX_DEV>(v, ro, dst + (unsigned)row * 4096 + (lane & 15) * 16);
    }
}

__device__ __forceinline__ void chain_b(LAS char* lds, int g, int i16, int wid, int lane, const char* bx, const char* cr, const char* ci,
                                        const char* hr, const char* hi, char* out, char* ws) {
    const int part = i16 >> 3, dt = i16 & 7;
    const int hh = i16 & 1, et = i16 >> 1;
    char* fl = ws + WS_FLAG;
    if (wid >= 4) {
        const int w4 = wid - 4;
        u64 ep = 0;
        const rsrc_t ru = mk(ws + WS_U, 8u << 20), rh0 = mk(hr, 1024 * 1024 * 2), rh1 = mk(hi, 1024 * 1024 * 2);
        const unsigned vl = lane * 16;
        const unsigned vh0 = vo_rows(lane, 2048), vh1 = vh0 ^ 64;
        const unsigned su = (unsigned)(g * 16 + hh * 8 + w4 * 2) * 65536;
        const unsigned sh = (unsigned)(et * 128 + w4 * 32) * 2048;
        const int rot2 = et + 2 * (hh * 2 + (g & 1));
        const int ua = (i16 >> 2) * 8 + ((2 * i16 - rot2) & 7), ub = (i16 >> 2) * 8 + ((2 * i16 + 1 - rot2) & 7);
        const int u_lo = ua < ub ? ua : ub, u_hi = ua < ub ? ub : ua;
        auto order2 = [&](int t) {
            int u = t - 2;
            u += u >= u_lo;
            u += u >= u_hi;
            return t < 2 ? 2 * i16 + t : kmap<32>(u, rot2);
        };
        auto issue_h = [&](int k0, int slot) {
            const int k = order2(k0);
            LAS char* l = lds + 65536 + slot * 16384 + w4 * 4096;
            const rsrc_t rh = k < 16 ? rh0 : rh1;
#pragma unroll
            for (int j = 0; j < 4; ++j) dma<AUX_IN>(rh, l + j * 1024, (j & 1) ? vh1 : vh0, sh + (unsigned)j * 8 * 2048 + (unsigned)(k & 15) * 128);
        };
        auto issue_u = [&](int k0, int slot) {
            const int k = order2(k0);
            LAS char* l = lds + slot * 16384 + w4 * 4096;
#pragma unroll
            for (int j = 0; j < 4; ++j) dma<AUX_I>(ru, l + j * 1024, vl, su + (unsigned)(j >> 1) * 65536 + (unsigned)(k * 2 + (j & 1)) * 1024);
        };
        {
            const rsrc_t rc = mk(part ? ci : cr, 2048 * 2048 * 2), rx = mk(bx, 2048 * 1024 * 2);
            const unsigned va0 = vo_rows(lane, 4096), va1 = va0 ^ 64;
            const unsigned vb0 = vo_tr(lane, (lane >> 3) * 2048), vb1 = vb0 ^ 64;
            const unsigned sa = (unsigned)(g * 256 + w4 * 64) * 4096;
            const unsigned sb = (unsigned)((w4 & 1) * 32) * 2048 + (unsigned)(dt * 128 + (w4 >> 1) * 64) * 2;
            const int tab = front_order<32, FRONT>(lane & 31, dt + 2 * (part * 2 + (g & 1)) + ((g >> 1) & 1));
            ep = ld_dev_late(fl + FL_EB + g * 128, tab);
            loader_loop<3, 12, 32, 8>([&](int k0, int slot) {
                const int k = __builtin_amdgcn_readlane(tab, k0);
                LAS char* la = lds + slot * 32768 + w4 * 8192;
                LAS char* lb = lds + 98304 + slot * 16384 + w4 * 4096;
#pragma unroll
                for (int j = 0; j < 4; ++j) dma<AUX_IN>(rx, lb + j * 1024, (j & 1) ? vb1 : vb0, sb + (unsigned)(k * 64 + j * 8) * 2048);
#pragma unroll
                for (int j = 0; j < 8; ++j) dma<AUX_IN>(rc, la + j * 1024, (j & 1) ? va1 : va0, sa + (unsigned)j * 8 * 4096 + (unsigned)k * 128);
            }, [&]() {
                issue_h(0, 0);
                issue_h(1, 1);
            });
        }
        WAIT_V(0);
        BAR();
        if (w4 == 0) announce(fl, FL_U, g, i16, mix(ep), lane);
#pragma unroll
        for (int s = 2; s < 4; ++s) issue_h(s, s);
        if (w4 == 0) {
            const u64 tag = mix(ep);
            poll(wait_line(fl, FL_U, g, i16), 16, tag, lane);
            if (i16 == 0 && lane == 0) st_dev(fl + FL_EB + g * 128, tag);
        }
        BAR();
        issue_u(2, 2);
        issue_u(3, 3);
        WAIT_V(4);
        BAR();
        issue_h(4, 0); issue_u(4, 0);
        issue_h(5, 1); issue_u(5, 1);
        loader_steady<4, 8, 32>([&](int k, int slot) { issue_h(k, slot); issue_u(k, slot); }, 2, 2);
        BAR();
        stage_store(lds, out, (unsigned)(g * 256 + hh * 128) * 4096 + 2048 + et * 256, wid, lane);
    } else {
        const int wm = wid >> 1, wn = wid & 1, fr = lane & 15, fq = lane >> 4;
        {
            f32x4 acc[8][4];
            bf16x8 a0[8], b0[4];
            zero_acc(acc);
            const int ra0 = (wm * 128 + fr) * 128 + ((fq ^ (fr >> 1)) * 16), ra1 = ra0 ^ 64;
            LAS char* pa[2][2] = {{pin(lds + ra0), pin(lds + ra1)}, {pin(lds + 65536 + ra0), pin(lds + 65536 + ra1)}};
            LAS char* pb[4];
#pragma unroll
            for (int c = 0; c < 4; ++c) pb[c] = pin(lds + 98304 + wn * 8192 + tr_base(lane, c));
            auto rdA = [&](int s, int kh, int m) { return ld128(pa[s >> 1][kh] + (s & 1) * 32768 + m * 2048); };
            auto rdB = [&](int s, int kh, int n, int t) { return ldtr(pb[n] + s * 16384 + kh * 4096 + t * 512); };
            BAR();
            first_frags(a0, b0, rdA, rdB);
            const rsrc_t ru = mk(ws + WS_U, 8u << 20);
            const unsigned so = (unsigned)((g * 16 + wm * 8) * 64 + part * 32 + dt * 4 + wn * 2) * 1024;
            compute_loop<8, 4, 4, 32, 3, 0>(acc, a0, b0, rdA, rdB);
            const unsigned sg = part ? 0x80008000u : 0u;
#pragma unroll
            for (int m = 0; m < 8; ++m)
#pragma unroll
                for (int np = 0; np < 2; ++np) {
                    u32x4 v = pk8(acc[m][2 * np], acc[m][2 * np + 1]);
                    v.x ^= sg; v.y ^= sg; v.z ^= sg; v.w ^= sg;
                    st128<AUX_DEV>(v, ru, lane * 16 + so + (unsigned)(m * 64 + np) * 1024);
                    if (wm == hh) *(LAS u32x4*)(lds + wn * 16384 + m * 2048 + np * 1024 + lane * 16) = v;
                }
            WAIT_L(0);
            WAIT_V(0);
        }
        BAR();
        {
            f32x4 acc[4][4];
            bf16x8 a0[4], b0[4];
            zero_acc(acc);
            LAS char* pa = pin(lds + wm * 8192 + lane * 16);
            LAS char* pb[4];
#pragma unroll
            for (int c = 0; c < 4; ++c) pb[c] = pin(lds + 65536 + (wn * 64 + fr) * 128 + (((c * 2 + (fq >> 1)) ^ (fr >> 1)) * 16) + 8 * (fq & 1));
            auto rdA = [&](int s, int kh, int m) { return ld128(pa + s * 16384 + m * 2048 + kh * 1024); };
            auto rdB = [&](int s, int kh, int n, int t) { return ld64(pb[kh * 2 + t] + s * 16384 + n * 2048); };
            resident_steps<4, 4, 4, 2>(acc, rdA, rdB);
            BAR();
            BAR();
            first_frags<2>(a0, b0, rdA, rdB);
            compute_loop<4, 4, 4, 30, 4, 2>(acc, a0, b0, rdA, rdB);
            stage_write(lds, acc, wm, wn, lane);
            WAIT_L(0);
        }
        BAR();
        stage_store(lds, out, (unsigned)(g * 256 + hh * 128) * 4096 + 2048 + et * 256, wid, lane);
    }
}

template <bool FIRST, int PAR>
__device__ __forceinline__ u32x4 attn_strip(LAS char* img, int s, bf16x8 qf, int lane) {
    constexpr float C = 0.17677669529663687f * 1.4426950408889634f;
    constexpr int JL = PAR, JD = PAR + 16;
    LAS char* p = img + (s & ~1) * 4096;
    const int r = lane & 15, g4 = (lane >> 4) * 4;
    f32x4 sc[18];
#pragma unroll
    for (int j = JL; j <= JD; ++j) sc[j] = MFMA(ld128(p + j * 4096), qf, ((f32x4){0.f, 0.f, 0.f, 0.f}));
    const float ninf = -__builtin_inff();
    f32x4 mg;
#pragma unroll
    for (int e = 0; e < 4; ++e) mg[e] = (g4 + e > r) ? (FIRST ? ninf : sc[JL][e]) : sc[JD][e];
    if (FIRST) {
#pragma unroll
        for (int j = JL + 1; j < JD; ++j) {
            const bool bad = (s & ~1) + j < 16;
#pragma unroll
            for (int e = 0; e < 4; ++e) sc[j][e] = bad ? ninf : sc[j][e];
        }
    }
    float mx = fmaxf(fmaxf(mg[0], mg[1]), fmaxf(mg[2], mg[3]));
#pragma unroll
    for (int j = JL + 1; j < JD; ++j) {
        mx = fmaxf(fmaxf(mx, sc[j][0]), sc[j][1]);
        mx = fmaxf(fmaxf(mx, sc[j][2]), sc[j][3]);
    }
    {
        auto a = __builtin_amdgcn_permlane16_swap(__float_as_uint(mx), __float_as_uint(mx), false, false);
        mx = fmaxf(__uint_as_float(a[0]), __uint_as_float(a[1]));
        auto b = __builtin_amdgcn_permlane32_swap(__float_as_uint(mx), __float_as_uint(mx), false, false);
        mx = fmaxf(__uint_as_float(b[0]), __uint_as_float(b[1]));
    }
    const float mc = -mx * C;
    const f32x2 c2 = {C, C}, m2 = {mc, mc};
    auto expo = [&](f32x4& v) {
        const f32x2 a = __builtin_elementwise_fma((f32x2){v[0], v[1]}, c2, m2), b = __builtin_elementwise_fma((f32x2){v[2], v[3]}, c2, m2);
        v[0] = __builtin_amdgcn_exp2f(a[0]); v[1] = __builtin_amdgcn_exp2f(a[1]);
        v[2] = __builtin_amdgcn_exp2f(b[0]); v[3] = __builtin_amdgcn_exp2f(b[1]);
    };
    expo(mg);
#pragma unroll
    for (int j = JL + 1; j < JD; ++j) expo(sc[j]);
#pragma unroll
    for (int e = 0; e < 4; ++e) {
        const bool lo = g4 + e > r;
        sc[JL][e] = lo ? mg[e] : 0.f;
        sc[JD][e] = lo ? 0.f : mg[e];
    }
    sc[PAR ? 0 : 17] = (f32x4){0.f, 0.f, 0.f, 0.f};
    f32x4 o0 = {0.f, 0.f, 0.f, 0.f}, o1 = o0, sum = o0;
    const bf16x8 ones = {0x3F80, 0x3F80, 0x3F80, 0x3F80, 0x3F80, 0x3F80, 0x3F80, 0x3F80};
#pragma unroll
    for (int j2 = 0; j2 < 9; ++j2) {
        const bf16x8 pf = __builtin_bit_cast(bf16x8, pk8(sc[2 * j2], sc[2 * j2 + 1]));
        o0 = MFMA(ld128(p + (2 * j2) * 4096 + 2048), pf, o0);
        o1 = MFMA(ld128(p + (2 * j2 + 1) * 4096 + 2048), pf, o1);
        sum = MFMA(ones, pf, sum);
    }
    const float inv = 1.0f / sum[0];
    o0 *= inv;
    o1 *= inv;
    return pk8(o0, o1);
}

template <bool FIRST>
__device__ __forceinline__ void attention(LAS char* lds, int r, int hp, int wid, int lane, char* ws, const char* wo, u64 tag_in, unsigned pf0,
                                          unsigned pf1) {
    const int h = wid & 1, sp = wid >> 1;
    constexpr int q1 = 16384, q0 = 0;
    const int head = hp * 2 + h;
    const rsrc_t ro = mk(ws + WS_O, 4u << 20);
    LAS char* img = lds + 32768 + h * 1024 + lane * 16;
    LAS char* qb = lds + (sp * 4 + h) * 1024 + lane * 16;
    const unsigned so = lane * 16 + (unsigned)((r * 16 + sp * 2) * 32 + head) * 1024;
    const u32x4 v0 = attn_strip<FIRST, 0>(img, 8 + sp * 2, ld128(qb + q1), lane);
    if (!FIRST && wid >= 4) {
        poll(ws + WS_FLAG + FL_KV + ((r - 1) * 16 + hp) * 64 + 32, 4, tag_in, lane);
        const rsrc_t rkv = mk(ws + WS_KV, 8u << 20);
        const unsigned src = (unsigned)(hp * 128 + r * 16 - 16) * 4096 + (unsigned)(wid - 4) * 8192;
#pragma unroll
        for (int j = 0; j < 8; ++j) dma<AUX_DEV>(rkv, lds + 32768 + (wid - 4) * 8192 + j * 1024, lane * 16, src + j * 1024);
    }
    st128<AUX_DEV>(v0, ro, so + 8 * 32768);
    const u32x4 v1 = attn_strip<FIRST, 1>(img, 8 + sp * 2 + 1, ld128(qb + q1 + 2048), lane);
    st128<AUX_DEV>(v1, ro, so + 9 * 32768);
    WAIT_V(2);
    asm volatile("" ::"v"(pf0), "v"(pf1));
    BAR();
    const u32x4 v2 = attn_strip<FIRST, 0>(img, sp * 2, ld128(qb + q0), lane);
    st128<AUX_DEV>(v2, ro, so);
    const u32x4 v3 = attn_strip<FIRST, 1>(img, sp * 2 + 1, ld128(qb + q0 + 2048), lane);
    st128<AUX_DEV>(v3, ro, so + 32768);
    WAIT_V(0);
}

__device__ __forceinline__ void chain_a(LAS char* lds, int r, int hp, int wid, int lane, const char* ax, const char* wqkv, const char* wo,
                                        const char* hr, const char* hi, const char* cr, const char* ci, const char* bx, char* out, char* ws) {
    char* fl = ws + WS_FLAG;
    u64 ep0 = 0;
    u64 tag_in, tag_out = 0;
    unsigned pf0 = 0, pf1 = 0;
    auto prefetch_later = [&]() {
        const int a = r * 16 + hp;
        pf0 = __builtin_amdgcn_raw_buffer_load_b32(mk(a < 64 ? hr : hi, 1024 * 1024 * 2), lane * 128, (unsigned)((a & 63) * 32768 + wid * 8192), 0);
        if (wid < 2) {
            const int i = (r * 4 + (hp & 3)) * 128 + wid * 64 + lane;
            pf1 = __builtin_amdgcn_raw_buffer_load_b32(mk(wo, 1024 * 1024 * 2), (unsigned)(i >> 2) * 2048 + (unsigned)(hp >> 2) * 512 + (i & 3) * 128, 0, 0);
        }
    };
    if (wid >= 4) {
        const int w4 = wid - 4;
        const rsrc_t rx = mk(ax, 2048 * 1024 * 2), rw = mk(wqkv, 1024 * 3072 * 2);
        const unsigned va0 = vo_rows(lane, 2048), va1 = va0 ^ 64;
        const unsigned vb0 = vo_tr(lane, (lane >> 3) * 6144), vb1 = vb0 ^ 64;
        const unsigned sa = (unsigned)(r * 256 + w4 * 32) * 2048;
        unsigned sb[6];
#pragma unroll
        for (int j = 0; j < 6; ++j) {
            const int i = w4 * 6 + j;
            sb[j] = (unsigned)((i & 7) * 8) * 6144 + (unsigned)((i >> 3) * 1024 + hp * 64) * 2;
        }
        const int tab = front_order<16, FRONT>(lane & 15, r + 2 * (hp & 3) + ((hp >> 2) & 1));
        ep0 = ld_dev_late(fl + FL_EA + r * 128, tab);
        loader_loop<3, 10, 32>([&](int k, int slot) {
            LAS char* la = lds + slot * 16384 + w4 * 4096;
            LAS char* lb = lds + 49152 + slot * 24576 + w4 * 6144;
            const unsigned kk = __builtin_amdgcn_readlane(tab, k & 15), sub = (k >> 4) ^ 1;
#pragma unroll
            for (int j = 0; j < 6; ++j) dma<AUX_IN>(rw, lb + j * 1024, (j & 1) ? vb1 : vb0, sb[j] + kk * 64 * 6144);
#pragma unroll
            for (int j = 0; j < 4; ++j) dma<AUX_IN>(rx, la + j * 1024, (j & 1) ? va1 : va0, sa + (sub * 128 + j * 8) * 2048 + kk * 128);
        });
        tag_in = uni64(mix(ep0));
        if (r > 0) {
            poll(fl + FL_KV + ((r - 1) * 16 + hp) * 64, 4, tag_in, lane);
            const rsrc_t rkv = mk(ws + WS_KV, 8u << 20);
            const unsigned src = (unsigned)(hp * 128 + r * 16 - 8) * 4096 + (unsigned)w4 * 8192;
#pragma unroll
            for (int j = 0; j < 8; ++j) dma<AUX_DEV>(rkv, lds + 65536 + w4 * 8192 + j * 1024, lane * 16, src + j * 1024);
        } else {
#pragma unroll
            for (int j = 0; j < 16; ++j) *(LAS u32x4*)(lds + 32768 + (w4 * 16 + j) * 1024 + lane * 16) = (u32x4){0u, 0u, 0u, 0u};
        }
        WAIT_V(0);
        WAIT_L(0);
        BAR();
    } else {
        ep0 = ld_dev_late(fl + FL_EA + r * 128);
        const u64 ep1 = ld_dev(fl + FL_EA + ((r + 1) & 7) * 128);
        tag_in = uni64(mix(ep0));
        tag_out = uni64(mix(ep1));
        const int wm = wid >> 1, h = wid & 1, fr = lane & 15, fq = lane >> 4;
        f32x4 acc[4][6];
        bf16x8 a0[4], b0[6];
        zero_acc(acc);
        const int ra0 = (wm * 64 + fr) * 128 + ((fq ^ (fr >> 1)) * 16);
        LAS char* pa[2] = {pin(lds + ra0), pin(lds + (ra0 ^ 64))};
        const int rb0 = 49152 + tr_base(lane, 2 * h), rb1 = 49152 + tr_base(lane, 2 * h + 1);
        LAS char* pb[2][2] = {{pin(lds + rb0), pin(lds + rb1)}, {pin(lds + 49152 + rb0), pin(lds + 49152 + rb1)}};
        auto rdA = [&](int s, int kh, int m) { return ld128(pa[kh] + s * 16384 + m * 2048); };
        auto rdB = [&](int s, int kh, int n, int t) { return ldtr(pb[s >> 1][n & 1] + (s & 1) * 24576 + (n >> 1) * 8192 + kh * 4096 + t * 512); };
        const rsrc_t rkv = mk(ws + WS_KV, 8u << 20);
        LAS char* img = lds + 32768 + 65536 + wm * 16384 + h * 1024 + lane * 16;
        const unsigned gkv = lane * 16 + (unsigned)(hp * 128 + r * 16 + wm * 4) * 4096 + (unsigned)h * 1024;
        char* fkv = fl + FL_KV + (r * 16 + hp) * 64 + wid * 8;
        auto put_kv = [&](int half) {
#pragma unroll
            for (int m = 0; m < 4; ++m) {
                const u32x4 kf = pk8(acc[m][2], acc[m][3]);
                const u32x4 vf = (m & 1) ? pk8(acc[m - 1][5], acc[m][5]) : pk8(acc[m][4], acc[m + 1 < 4 ? m + 1 : 3][4]);
                const unsigned o = (unsigned)(half * 8 + m) * 4096;
                if (r < 7) {
                    st128<AUX_DEV>(kf, rkv, gkv + o);
                    st128<AUX_DEV>(vf, rkv, gkv + o + 2048);
                }
                *(LAS u32x4*)(img + o) = kf;
                *(LAS u32x4*)(img + o + 2048) = vf;
            }
        };
        BAR();
        first_frags(a0, b0, rdA, rdB);
        compute_loop<4, 6, 4, 16, 3, 0>(acc, a0, b0, rdA, rdB);
        put_kv(1);
        u32x4 q1[4];
#pragma unroll
        for (int m = 0; m < 4; ++m) q1[m] = pk8(acc[m][0], acc[m][1]);
        zero_acc(acc);
        compute_loop<4, 6, 4, 6, 3, 1>(acc, a0, b0, rdA, rdB);
        WAIT_V(0);
        if (r < 7 && lane == 0) st_dev(fkv, tag_out);
        prefetch_later();
        compute_loop<4, 6, 4, 10, 3, 1>(acc, a0, b0, rdA, rdB);
        put_kv(0);
        LAS char* qb = lds + (wm * 8 + h) * 1024 + lane * 16;
#pragma unroll
        for (int m = 0; m < 4; ++m) {
            *(LAS u32x4*)(qb + m * 2048) = pk8(acc[m][0], acc[m][1]);
            *(LAS u32x4*)(qb + 16384 + m * 2048) = q1[m];
        }
        WAIT_L(0);
        BAR();
        WAIT_V(0);
        if (r < 7 && lane == 0) st_dev(fkv + 32, tag_out);
    }
    if (r > 0) attention<false>(lds, r, hp, wid, lane, ws, wo, tag_in, pf0, pf1);
    else attention<true>(lds, r, hp, wid, lane, ws, wo, tag_in, pf0, pf1);
    BAR();
    const int hh = hp & 1, ct = hp >> 1;
    const unsigned dst = (unsigned)(r * 256 + hh * 128) * 4096 + ct * 256;
    if (wid >= 4) {
        const int w4 = wid - 4;
        const rsrc_t ro = mk(ws + WS_O, 4u << 20), rw = mk(wo, 1024 * 1024 * 2);
        const unsigned vl = lane * 16;
        const unsigned vb0 = vo_tr(lane, (unsigned)(16 * (lane >> 5) + ((lane >> 3) & 3)) * 2048), vb1 = vb0 ^ 64;
        const unsigned so = (unsigned)(r * 16 + hh * 8 + w4 * 2) * 32768;
        const unsigned sb = (unsigned)((w4 & 1) * 32) * 2048 + (unsigned)(ct * 128 + (w4 >> 1) * 64) * 2;
        const int rot = r + 4 * (ct & 1);
        auto issue_w = [&](int k0, int slot) {
            const int k = kmap<16>(k0, rot);
            LAS char* l = lds + 65536 + slot * 16384 + w4 * 4096;
#pragma unroll
            for (int j = 0; j < 4; ++j) dma<AUX_IN>(rw, l + j * 1024, (j & 1) ? vb1 : vb0, sb + (unsigned)(k * 64 + j * 4) * 2048);
        };
        auto issue_o = [&](int k0, int slot) {
            const int k = kmap<16>(k0, rot);
            LAS char* l = lds + slot * 16384 + w4 * 4096;
#pragma unroll
            for (int j = 0; j < 4; ++j) dma<AUX_I>(ro, l + j * 1024, vl, so + (unsigned)(j >> 1) * 32768 + (unsigned)(k * 2 + (j & 1)) * 1024);
        };
        if (w4 == 0) announce(fl, FL_O, r, hp, tag_in, lane);
#pragma unroll
        for (int s = 0; s < 4; ++s) issue_w(s, s);
        if (w4 == 0) {
            poll(wait_line(fl, FL_O, r, hp), 16, tag_in, lane);
            if (hp == 0 && lane == 0) st_dev(fl + FL_EA + r * 128, tag_in);
        }
        BAR();
        loader_tail<4, 4, 4, 16>([&](int k, int slot) { issue_w(k, slot); issue_o(k, slot); }, issue_o);
        BAR();
        stage_store(lds, out, dst, wid, lane);
    } else {
        const int wm = wid >> 1, wn = wid & 1;
        f32x4 acc[4][4];
        bf16x8 a0[4], b0[4];
        zero_acc(acc);
        LAS char* pa = pin(lds + wm * 8192 + lane * 16);
        LAS char* pb[4];
#pragma unroll
        for (int c = 0; c < 4; ++c) pb[c] = pin(lds + 65536 + wn * 8192 + tr_base(lane, c));
        auto rdA = [&](int s, int kh, int m) { return ld128(pa + s * 16384 + m * 2048 + kh * 1024); };
        auto rdB = [&](int s, int kh, int n, int t) { return ldtr(pb[n] + s * 16384 + kh * 4096 + t * 512); };
        BAR();
        BAR();
        first_frags(a0, b0, rdA, rdB);
        compute_loop<4, 4, 4, 16, 4, 0>(acc, a0, b0, rdA, rdB);
        stage_write(lds, acc, wm, wn, lane);
        WAIT_L(0);
        BAR();
        stage_store(lds, out, dst, wid, lane);
    }
}

__global__ __launch_bounds__(512, 2) void fused_kernel(const char* ax, const char* wqkv, const char* wo, const char* bx, const char* cr,
                                                       const char* ci, const char* hr, const char* hi, char* out, char* ws) {
    extern __shared__ __attribute__((aligned(16))) char smem[];
    LAS char* lds = (LAS char*)smem;
    const int wid = __builtin_amdgcn_readfirstlane(threadIdx.x >> 6), lane = threadIdx.x & 63;
    const int x = blockIdx.x & 7, idx = blockIdx.x >> 3;
    const int j = (XJ >> (2 * x)) & 3;
    const bool isb = (XB >> x) & 1;
    asm volatile(".Lfk_begin:");
    if (wid == 0) touch_code(isb ? 147456 : 122880, idx, lane);
    if (isb) {
        chain_b(lds, 2 * j + (idx >> 4), idx & 15, wid, lane, bx, cr, ci, hr, hi, out, ws);
        asm volatile(".Lfk_end_b:");
    } else {
        chain_a(lds, idx >> 2, 4 * j + (idx & 3), wid, lane, ax, wqkv, wo, hr, hi, cr, ci, bx, out, ws);
        asm volatile(".Lfk_end_a:");
    }
}

}

extern "C" void kernel_launch(void* const* d_in, const int* in_sizes, int n_in, void* d_out, int out_size, void* d_ws, size_t ws_size,
                              hipStream_t stream) {
    (void)in_sizes; (void)n_in; (void)out_size;
    (void)hipFuncSetAttribute((const void*)fused_kernel, hipFuncAttributeMaxDynamicSharedMemorySize, LDS_BYTES);
    fused_kernel<<<256, 512, LDS_BYTES, stream>>>((const char*)d_in[0], (const char*)d_in[1], (const char*)d_in[2], (const char*)d_in[3],
                                                  (const char*)d_in[4], (const char*)d_in[5], (const char*)d_in[6], (const char*)d_in[7],
                                                  (char*)d_out, (char*)d_ws);
}
```
